```python
import torch

def _softmax(x, axis=-1):
    return torch.softmax(x, dim=axis)
def _gelu(x):
    return torch.nn.functional.gelu(x, approximate="tanh")
def _silu(x):
    return torch.nn.functional.silu(x)
def _sigmoid(x):
    return torch.sigmoid(x)
def _layernorm(x, axis=-1, eps=1e-5):
    m = x.mean(dim=axis, keepdim=True)
    v = x.var(dim=axis, keepdim=True, unbiased=False)
    return (x - m) / torch.sqrt(v + eps)
def _scatter_add(tgt, idx, src, axis=0):
    return tgt.clone().index_add_(axis, idx, src)
def _segment_sum(src, seg, n):
    out = torch.zeros((n,) + src.shape[1:], dtype=src.dtype)
    return out.index_add_(0, seg, src)
def _pad(x, pw):
    flat = tuple(v for pair in reversed(pw) for v in pair)
    return torch.nn.functional.pad(x, flat)

kernel_name = 'standalone__glu_gated_geglu__prompt'

def setup_inputs():

    _w = (lambda a: (lambda t: t.to(torch.bfloat16) if t.is_floating_point() else t)(torch.as_tensor(a)))
    B, S, Dm, Df = 1, 2048, 512, 1376
    return {
        "x": _w((lambda *s: torch.randn(*s, dtype=torch.bfloat16) * 0.05)(B, S, Dm)),
        "w_gate": _w((lambda *s: torch.randn(*s, dtype=torch.bfloat16) * 0.05)(Dm, Df) * (Dm ** -0.5)),
        "w_up": _w((lambda *s: torch.randn(*s, dtype=torch.bfloat16) * 0.05)(Dm, Df) * (Dm ** -0.5)),
        "w_down": _w((lambda *s: torch.randn(*s, dtype=torch.bfloat16) * 0.05)(Df, Dm) * (Df ** -0.5)),
    }

def reference(x, w_gate, w_up, w_down):
    g = torch.einsum("bsd,df->bsf", x, w_gate)
    u = torch.einsum("bsd,df->bsf", x, w_up)
    h = _gelu(g) * u
    return torch.einsum("bsf,fd->bsd", h, w_down)
```

```cpp
#include <hip/hip_runtime.h>
#include <hip/hip_bf16.h>

typedef unsigned short u16;
typedef unsigned int u32;
typedef __bf16 bf16x8 __attribute__((ext_vector_type(8)));
typedef short s16x4 __attribute__((ext_vector_type(4)));
typedef short s16x8 __attribute__((ext_vector_type(8)));
typedef float f32x16 __attribute__((ext_vector_type(16)));
typedef float f32x4 __attribute__((ext_vector_type(4)));
typedef float f32x2 __attribute__((ext_vector_type(2)));
typedef __bf16 bf16x2 __attribute__((ext_vector_type(2)));
typedef u32 u32x4 __attribute__((ext_vector_type(4)));
typedef __attribute__((address_space(3))) void* lds_vptr;
typedef __attribute__((address_space(3))) s16x4* lds_s16x4p;
typedef __attribute__((address_space(3))) s16x8* lds_s16x8p;
typedef __attribute__((address_space(3))) f32x4* lds_f32x4p;

constexpr int SEQ = 2048, DM = 512, DF = 1376;
constexpr int NKS = DF / 16;
constexpr int WPITCH = DF * 2;
constexpr int SLOT = 40960;

__device__ __forceinline__ void dma16(__amdgpu_buffer_rsrc_t rsrc, u32 lds_addr, u32 voff, u32 soff) {
  __builtin_amdgcn_raw_ptr_buffer_load_lds(rsrc, (lds_vptr)(size_t)lds_addr, 16, voff, soff, 0, 0);
}
__device__ __forceinline__ bf16x8 lds_b128(u32 a) { return __builtin_bit_cast(bf16x8, *(lds_s16x8p)(size_t)a); }
__device__ __forceinline__ bf16x8 lds_tr2(u32 a0, u32 a1) {
  s16x4 lo = __builtin_amdgcn_ds_read_tr16_b64_v4i16((lds_s16x4p)(size_t)a0);
  s16x4 hi = __builtin_amdgcn_ds_read_tr16_b64_v4i16((lds_s16x4p)(size_t)a1);
  return __builtin_bit_cast(bf16x8, __builtin_shufflevector(lo, hi, 0, 1, 2, 3, 4, 5, 6, 7));
}
#define WAIT_V(n) asm volatile("s_waitcnt vmcnt(" #n ")" ::: "memory")
#define BAR() __builtin_amdgcn_s_barrier()

__device__ __forceinline__ float geglu1(float g, float u) {
  const float c0 = -2.0f * 0.7978845608028654f * 1.4426950408889634f;
  const float c1 = c0 * 0.044715f;
  float z = g * __builtin_fmaf(g * g, c1, c0);
  float e = __builtin_amdgcn_exp2f(z);
  float r = __builtin_amdgcn_rcpf(1.0f + e);
  return g * r * u;
}
__device__ __forceinline__ u32 pack_bf16(float a, float b) {
  f32x2 v = {a, b};
  return __builtin_bit_cast(u32, __builtin_convertvector(v, bf16x2));
}

__global__ __launch_bounds__(512, 2) void k1(const u16* __restrict__ x, const u16* __restrict__ wg,
                                             const u16* __restrict__ wu, u32x4* __restrict__ hfrag) {
  extern __shared__ __attribute__((aligned(16))) char smem[];
  const int lane = threadIdx.x & 63;
  const int wid = __builtin_amdgcn_readfirstlane(threadIdx.x >> 6);
  const int b = blockIdx.x, g = b & 7, r = b >> 3;
  const int mi = (g >> 1) * 4 + (r & 3), nj = (g & 1) * 8 + (r >> 2);
  if (nj >= 15) return;
  const int rot = ((r & 3) * 2 + (r >> 2)) & 7;
  const u32 lds0 = (u32)(size_t)smem;

  if (wid >= 4) {
    const int lw = wid - 4;
    __amdgpu_buffer_rsrc_t rx = __builtin_amdgcn_make_buffer_rsrc((void*)x, 0, SEQ * DM * 2, 0x00020000);
    __amdgpu_buffer_rsrc_t rg = __builtin_amdgcn_make_buffer_rsrc((void*)wg, 0, DM * DF * 2, 0x00020000);
    __amdgpu_buffer_rsrc_t ru = __builtin_amdgcn_make_buffer_rsrc((void*)wu, 0, DM * DF * 2, 0x00020000);
    u32 vx[2];
#pragma unroll
    for (int e = 0; e < 2; ++e) {
      int row = e * 8 + (lane >> 3);
      vx[e] = (u32)(row * 1024 + (((lane & 7) ^ ((row >> 1) & 7)) << 4));
    }
    u32 vg[3], vu[3];
#pragma unroll
    for (int j = 0; j < 3; ++j) {
      int c = j * 64 + lane, row = c / 12, ch = c % 12;
      vg[j] = (u32)(row * WPITCH + ch * 16);
      vu[j] = (u32)(row * WPITCH + (ch ^ 2) * 16);
    }
    const u32 sx0 = (u32)((mi * 128 + lw * 32) * 1024);
    const u32 sw0 = (u32)(lw * 16 * WPITCH + nj * 192);
    auto issue = [&](int s) {
      const int kt = (s + rot) & 7;
      const u32 base = lds0 + (u32)(s & 3) * SLOT;
      const u32 sx = sx0 + kt * 128, sw = sw0 + kt * 64 * WPITCH;
      dma16(rx, base + lw * 4096, vx[0], sx);
      dma16(rx, base + lw * 4096 + 1024, vx[1], sx);
      dma16(rx, base + lw * 4096 + 2048, vx[0], sx + 16384);
      dma16(rx, base + lw * 4096 + 3072, vx[1], sx + 16384);
#pragma unroll
      for (int j = 0; j < 3; ++j) dma16(rg, base + 16384 + lw * 3072 + j * 1024, vg[j], sw);
#pragma unroll
      for (int j = 0; j < 3; ++j) dma16(ru, base + 28672 + lw * 3072 + j * 1024, vu[j], sw);
    };
    issue(0); issue(1); issue(2); issue(3);
    WAIT_V(30); BAR();
    for (int s = 1; s <= 4; ++s) { WAIT_V(20); BAR(); issue(s + 3); }
    WAIT_V(20); BAR();
    WAIT_V(10); BAR();
    WAIT_V(0); BAR();
  } else {
    const int wm = wid >> 1, wn = wid & 1;
    const int hi = lane >> 5, r32 = lane & 31;
    u32 ax[4];
#pragma unroll
    for (int s = 0; s < 4; ++s) ax[s] = (u32)((wm * 64 + r32) * 128 + (((2 * s + hi) ^ ((r32 >> 1) & 7)) << 4));
    const int half = (lane >> 4) & 1, q = (lane >> 2) & 3, p = lane & 3;
    const u32 awb = (u32)(16384 + half * 12288 + (8 * hi + q) * 192 + (8 * (p & 1) + 4 * (p >> 1)) * 2);
    u32 aw[3];
#pragma unroll
    for (int t = 0; t < 3; ++t) { int tt = wn * 3 + t; aw[t] = awb + (u32)((tt ^ half) * 32); }
    f32x16 acc[2][3] = {};
    for (int s = 0; s < 8; ++s) {
      BAR();
      const u32 base = lds0 + (u32)(s & 3) * SLOT;
#pragma unroll
      for (int ks = 0; ks < 4; ++ks) {
        bf16x8 xf[2], wf[3];
#pragma unroll
        for (int mb = 0; mb < 2; ++mb) xf[mb] = lds_b128(base + ax[ks] + mb * 4096);
#pragma unroll
        for (int t = 0; t < 3; ++t) wf[t] = lds_tr2(base + aw[t] + ks * 3072, base + aw[t] + ks * 3072 + 768);
#pragma unroll
        for (int mb = 0; mb < 2; ++mb)
#pragma unroll
          for (int t = 0; t < 3; ++t)
            acc[mb][t] = __builtin_amdgcn_mfma_f32_32x32x16_bf16(wf[t], xf[mb], acc[mb][t], 0, 0, 0);
      }
      asm volatile("s_waitcnt lgkmcnt(0)" ::: "memory");
    }
#pragma unroll
    for (int mb = 0; mb < 2; ++mb)
#pragma unroll
      for (int t = 0; t < 3; ++t) {
        const int ks = nj * 6 + wn * 3 + t;
        if (ks < NKS) {
          u32x4 o;
#pragma unroll
          for (int j = 0; j < 4; ++j)
            o[j] = pack_bf16(geglu1(acc[mb][t][2 * j], acc[mb][t][8 + 2 * j]), geglu1(acc[mb][t][2 * j + 1], acc[mb][t][9 + 2 * j]));
          hfrag[((size_t)(mi * 4 + wm * 2 + mb) * NKS + ks) * 64 + lane] = o;
        }
      }
  }
}

__global__ __launch_bounds__(512, 2) void k2(const u32x4* __restrict__ hfrag, const u16* __restrict__ wd, u16* __restrict__ out) {
  extern __shared__ __attribute__((aligned(16))) char smem[];
  const int lane = threadIdx.x & 63;
  const int wid = __builtin_amdgcn_readfirstlane(threadIdx.x >> 6);
  const int b = blockIdx.x, g = b & 7, r = b >> 3;
  const int mp = g * 4 + (r & 3), nt = r >> 2;
  const int kq = wid >> 1, mb = wid & 1;
  const u32 lds0 = (u32)(size_t)smem;
  const int ks0 = kq * 22;
  __amdgpu_buffer_rsrc_t rd = __builtin_amdgcn_make_buffer_rsrc((void*)wd, 0, DF * DM * 2, 0x00020000);
  __amdgpu_buffer_rsrc_t rh = __builtin_amdgcn_make_buffer_rsrc((void*)hfrag, 0, 64 * NKS * 1024, 0x00020000);
  const u32 vd = (u32)((lane >> 3) * 1024 + (((lane & 7) ^ (((lane >> 4) & 1) << 2)) << 4));
  const u32 sd0 = (u32)((ks0 * 16 + mb * 32) * 1024 + nt * 128);
  const u32 vh = (u32)(lane * 16);
  const u32 sh0 = (u32)(((mp * 2 + mb) * NKS + ks0) * 1024);
  u32x4 hf[22];
  auto issue_w = [&](int st) {
    const u32 base = lds0 + (u32)(st % 5) * 32768 + kq * 8192 + mb * 4096;
    const int n = (st == 5) ? 2 : 4;
#pragma unroll
    for (int i = 0; i < 4; ++i) {
      if (st == 5) { if (i < 2) dma16(rd, lds0 + kq * 8192 + mb * 2048 + i * 1024, vd, (u32)((ks0 * 16 + 320 + mb * 16 + i * 8) * 1024 + nt * 128)); }
      else dma16(rd, base + i * 1024, vd, sd0 + (u32)((st * 64 + i * 8) * 1024));
    }
    (void)n;
  };
#define HLOAD(i) do { u32 so = sh0 + (u32)((i) * 1024); if (ks0 + (i) >= NKS) so = 0x7ffffff0u; \
    asm volatile("buffer_load_dwordx4 %0, %1, %2, %3 offen" : "=v"(hf[i]) : "v"(vh), "s"(rh), "s"(so) : "memory"); } while (0)
#pragma unroll
  for (int st = 0; st < 5; ++st) {
    issue_w(st);
    HLOAD(st * 4 + 0); HLOAD(st * 4 + 1); HLOAD(st * 4 + 2); HLOAD(st * 4 + 3);
  }
  HLOAD(20); HLOAD(21);
  const int hi = lane >> 5, half = (lane >> 4) & 1, q = (lane >> 2) & 3, p = lane & 3;
  const u32 ad0 = (u32)(kq * 8192 + (8 * hi + q) * 128 + (((q >> 1) & 1) * 64) + half * 32 + p * 8);
  const u32 ad1 = ad0 ^ 64;
  f32x16 acc[2] = {};
#define STAGE_COMPUTE(st, nks) do { \
    const u32 base = lds0 + (u32)((st) % 5) * 32768; \
    _Pragma("unroll") for (int k = 0; k < (nks); ++k) { \
      bf16x8 w0 = lds_tr2(base + ad0 + k * 2048, base + ad0 + k * 2048 + 512); \
      bf16x8 w1 = lds_tr2(base + ad1 + k * 2048, base + ad1 + k * 2048 + 512); \
      bf16x8 hh = __builtin_bit_cast(bf16x8, hf[(st) * 4 + k]); \
      acc[0] = __builtin_amdgcn_mfma_f32_32x32x16_bf16(w0, hh, acc[0], 0, 0, 0); \
      acc[1] = __builtin_amdgcn_mfma_f32_32x32x16_bf16(w1, hh, acc[1], 0, 0, 0); \
    } \
    asm volatile("s_waitcnt lgkmcnt(0)" ::: "memory"); } while (0)
#define HW4(n, a) asm volatile("s_waitcnt vmcnt(" #n ")" : "+v"(hf[a]), "+v"(hf[a + 1]), "+v"(hf[a + 2]), "+v"(hf[a + 3]) :: "memory")
  HW4(34, 0); BAR(); STAGE_COMPUTE(0, 4);
  HW4(26, 4); BAR(); issue_w(5); STAGE_COMPUTE(1, 4);
  HW4(20, 8); BAR(); STAGE_COMPUTE(2, 4);
  HW4(12, 12); BAR(); STAGE_COMPUTE(3, 4);
  HW4(4, 16); BAR(); STAGE_COMPUTE(4, 4);
  asm volatile("s_waitcnt vmcnt(0)" : "+v"(hf[20]), "+v"(hf[21]) :: "memory"); BAR(); STAGE_COMPUTE(5, 2);
  BAR();
  {
    const u32 wb = lds0 + (u32)(mb * 32768 + kq * 8192 + lane * 16);
#pragma unroll
    for (int part = 0; part < 4; ++part) {
      if (part != kq) {
#pragma unroll
        for (int h2 = 0; h2 < 2; ++h2) {
          const int rb = (part & 1) * 8 + h2 * 4;
          f32x4 v = {acc[part >> 1][rb], acc[part >> 1][rb + 1], acc[part >> 1][rb + 2], acc[part >> 1][rb + 3]};
          *(lds_f32x4p)(size_t)(wb + part * 2048 + h2 * 1024) = v;
        }
      }
    }
    asm volatile("s_waitcnt lgkmcnt(0)" ::: "memory");
    BAR();
    f32x4 s0, s1;
#pragma unroll
    for (int part = 0; part < 4; ++part)
      if (part == kq) {
        const int rb = (part & 1) * 8;
        s0 = f32x4{acc[part >> 1][rb], acc[part >> 1][rb + 1], acc[part >> 1][rb + 2], acc[part >> 1][rb + 3]};
        s1 = f32x4{acc[part >> 1][rb + 4], acc[part >> 1][rb + 5], acc[part >> 1][rb + 6], acc[part >> 1][rb + 7]};
      }
#pragma unroll
    for (int d = 1; d < 4; ++d) {
      const int src = (kq + d) & 3;
      const u32 rb = lds0 + (u32)(mb * 32768 + src * 8192 + kq * 2048 + lane * 16);
      s0 += *(lds_f32x4p)(size_t)(rb);
      s1 += *(lds_f32x4p)(size_t)(rb + 1024);
    }
    u32 a0 = pack_bf16(s0[0], s0[1]), a1 = pack_bf16(s0[2], s0[3]);
    u32 b0 = pack_bf16(s1[0], s1[1]), b1 = pack_bf16(s1[2], s1[3]);
    auto r0 = __builtin_amdgcn_permlane32_swap(a0, b0, false, false);
    auto r1 = __builtin_amdgcn_permlane32_swap(a1, b1, false, false);
    u32x4 o = {r0[0], r1[0], r0[1], r1[1]};
    const int row = mp * 64 + mb * 32 + (lane & 31);
    *(u32x4*)(out + (size_t)row * DM + nt * 64 + kq * 16 + hi * 8) = o;
  }
}

extern "C" void kernel_launch(void* const* d_in, const int* in_sizes, int n_in, void* d_out, int out_size, void* d_ws,
                              size_t ws_size, hipStream_t stream) {
  const u16* x = (const u16*)d_in[0];
  const u16* wg = (const u16*)d_in[1];
  const u16* wu = (const u16*)d_in[2];
  const u16* wd = (const u16*)d_in[3];
  (void)hipFuncSetAttribute((const void*)k1, hipFuncAttributeMaxDynamicSharedMemorySize, 163840);
  (void)hipFuncSetAttribute((const void*)k2, hipFuncAttributeMaxDynamicSharedMemorySize, 163840);
  k1<<<256, 512, 163840, stream>>>(x, wg, wu, (u32x4*)d_ws);
  k2<<<256, 512, 163840, stream>>>((const u32x4*)d_ws, wd, (u16*)d_out);
}
```

```cpp
#include <hip/hip_runtime.h>
#include <hip/hip_bf16.h>

typedef unsigned short u16;
typedef unsigned int u32;
typedef unsigned long long u64;
typedef float f32x16 __attribute__((ext_vector_type(16)));
typedef float f32x4 __attribute__((ext_vector_type(4)));
typedef float f32x2 __attribute__((ext_vector_type(2)));
typedef __bf16 bf16x2 __attribute__((ext_vector_type(2)));
typedef u32 u32x4 __attribute__((ext_vector_type(4)));
typedef u32 u32x2 __attribute__((ext_vector_type(2)));
typedef __attribute__((address_space(3))) f32x4* lds_f32x4p;

constexpr int SEQ = 2048, DM = 512, DF = 1376;
constexpr int NKS = DF / 16;
constexpr int WPITCH = DF * 2;
constexpr int SLOT = 40960;

#define DMA16(rsrc, lds, voff, soff) \
  asm volatile("s_mov_b32 m0, %0\n\ts_nop 0\n\tbuffer_load_dwordx4 %1, %2, %3 offen lds" :: "s"(lds), "v"(voff), "s"(rsrc), "s"(soff) : "memory")
#define RD128(dst, addr, off) asm volatile("ds_read_b128 %0, %1 offset:%2" : "=v"(dst) : "v"(addr), "i"(off))
#define RDTR(dst, addr, off) asm volatile("ds_read_b64_tr_b16 %0, %1 offset:%2" : "=v"(dst) : "v"(addr), "i"(off))
#define MFMA(acc, a, b) asm volatile("v_mfma_f32_32x32x16_bf16 %0, %1, %2, %0" : "+v"(acc) : "v"(a), "v"(b))
#define WAIT_V(n) asm volatile("s_waitcnt vmcnt(" #n ")" ::: "memory")
#define WAIT_L(n) asm volatile("s_waitcnt lgkmcnt(" #n ")" ::: "memory")
#define BAR() asm volatile("s_barrier" ::: "memory")
#define CAT4(lo, hi) __builtin_shufflevector(lo, hi, 0, 1, 2, 3)

#define STAMP(i) do {} while (0)

__device__ __forceinline__ float geglu1(float g, float u) {
  const float c0 = -2.0f * 0.7978845608028654f * 1.4426950408889634f;
  const float c1 = c0 * 0.044715f;
  float z = g * __builtin_fmaf(g * g, c1, c0);
  float e = __builtin_amdgcn_exp2f(z);
  float r = __builtin_amdgcn_rcpf(1.0f + e);
  return g * r * u;
}
__device__ __forceinline__ u32 pack_bf16(float a, float b) {
  f32x2 v = {a, b};
  return __builtin_bit_cast(u32, __builtin_convertvector(v, bf16x2));
}

__global__ __launch_bounds__(512, 2) void k1(const u16* __restrict__ x, const u16* __restrict__ wg,
                                             const u16* __restrict__ wu, u32x4* __restrict__ hfrag, u64* __restrict__ diag) {
  extern __shared__ __attribute__((aligned(16))) char smem[];
  const int lane = threadIdx.x & 63;
  const int wid = __builtin_amdgcn_readfirstlane(threadIdx.x >> 6);
  const int b = blockIdx.x, g = b & 7, r = b >> 3;
  const int mi = (g >> 1) * 4 + (r & 3), nj = (g & 1) * 8 + (r >> 2);
  if (nj >= 15) return;
  const int rot = ((r & 3) * 2 + (r >> 2)) & 7;
  const u32 lds0 = (u32)(size_t)smem;
  STAMP(0);

  if (wid >= 4) {
    const int lw = wid - 4;
    __amdgpu_buffer_rsrc_t rx = __builtin_amdgcn_make_buffer_rsrc((void*)x, 0, SEQ * DM * 2, 0x00020000);
    __amdgpu_buffer_rsrc_t rg = __builtin_amdgcn_make_buffer_rsrc((void*)wg, 0, DM * DF * 2, 0x00020000);
    __amdgpu_buffer_rsrc_t ru = __builtin_amdgcn_make_buffer_rsrc((void*)wu, 0, DM * DF * 2, 0x00020000);
    u32 vx[2];
#pragma unroll
    for (int e = 0; e < 2; ++e) {
      int row = e * 8 + (lane >> 3);
      vx[e] = (u32)(row * 1024 + (((lane & 7) ^ ((row >> 1) & 7)) << 4));
    }
    u32 vg[3], vu[3];
#pragma unroll
    for (int j = 0; j < 3; ++j) {
      int c = j * 64 + lane, row = c / 12, ch = c % 12;
      vg[j] = (u32)(row * WPITCH + ch * 16);
      vu[j] = (u32)(row * WPITCH + (ch ^ 2) * 16);
    }
    const u32 sx0 = (u32)((mi * 128 + lw * 32) * 1024);
    const u32 sw0 = (u32)(lw * 16 * WPITCH + nj * 192);
#pragma unroll 1
    for (int s = 0; s < 13; ++s) {
      if (s >= 4) {
        if (s == 4) WAIT_V(30); else if (s < 10) WAIT_V(20); else if (s == 10) WAIT_V(10); else WAIT_V(0);
        BAR();
        STAMP(s - 3);
      }
      const int st = s < 4 ? s : s - 1;
      if (s != 4 && s <= 8) {
        const int kt = (st + rot) & 7;
        const u32 base = lds0 + (u32)(st & 3) * SLOT;
        const u32 sx = sx0 + kt * 128, sw = sw0 + kt * 64 * WPITCH;
        DMA16(rx, base + lw * 4096, vx[0], sx);
        DMA16(rx, base + lw * 4096 + 1024, vx[1], sx);
        DMA16(rx, base + lw * 4096 + 2048, vx[0], sx + 16384);
        DMA16(rx, base + lw * 4096 + 3072, vx[1], sx + 16384);
#pragma unroll
        for (int j = 0; j < 3; ++j) DMA16(rg, base + 16384 + lw * 3072 + j * 1024, vg[j], sw);
#pragma unroll
        for (int j = 0; j < 3; ++j) DMA16(ru, base + 28672 + lw * 3072 + j * 1024, vu[j], sw);
      }
    }
  } else {
    const int wm = wid >> 1, wn = wid & 1;
    const int hi = lane >> 5, r32 = lane & 31;
    u32 ax0, ax1, ax2, ax3, aw0, aw1, aw2;
    {
      const u32 rb = lds0 + (u32)((wm * 64 + r32) * 128), sw = (u32)((r32 >> 1) & 7);
      ax0 = rb + (((0 + hi) ^ sw) << 4); ax1 = rb + (((2 + hi) ^ sw) << 4);
      ax2 = rb + (((4 + hi) ^ sw) << 4); ax3 = rb + (((6 + hi) ^ sw) << 4);
      const int half = (lane >> 4) & 1, q = (lane >> 2) & 3, p = lane & 3;
      const u32 awb = lds0 + (u32)(16384 + half * 12288 + (8 * hi + q) * 192 + (8 * (p & 1) + 4 * (p >> 1)) * 2);
      aw0 = awb + (u32)(((wn * 3 + 0) ^ half) * 32);
      aw1 = awb + (u32)(((wn * 3 + 1) ^ half) * 32);
      aw2 = awb + (u32)(((wn * 3 + 2) ^ half) * 32);
    }
    f32x16 a00 = {}, a01 = {}, a02 = {}, a10 = {}, a11 = {}, a12 = {};
    u32 cnt, dd;
    asm volatile(
      "s_barrier\n\t"
      "ds_read_b64_tr_b16 v[208:209], %[aw0] offset:0\n\t"
      "ds_read_b64_tr_b16 v[210:211], %[aw0] offset:768\n\t"
      "ds_read_b128 v[200:203], %[ax0]\n\t"
      "ds_read_b64_tr_b16 v[212:213], %[aw1] offset:0\n\t"
      "ds_read_b64_tr_b16 v[214:215], %[aw1] offset:768\n\t"
      "ds_read_b64_tr_b16 v[216:217], %[aw2] offset:0\n\t"
      "ds_read_b64_tr_b16 v[218:219], %[aw2] offset:768\n\t"
      "ds_read_b128 v[204:207], %[ax0] offset:4096\n\t"
      "s_mov_b32 %[cnt], 0\n\t"
      "1:\n\t"
      "s_waitcnt lgkmcnt(0)\n\t"
      "v_mfma_f32_32x32x16_bf16 %[a00], v[208:211], v[200:203], %[a00]\n\t"
      "ds_read_b64_tr_b16 v[228:229], %[aw0] offset:3072\n\t"
      "ds_read_b64_tr_b16 v[230:231], %[aw0] offset:3840\n\t"
      "v_mfma_f32_32x32x16_bf16 %[a01], v[212:215], v[200:203], %[a01]\n\t"
      "ds_read_b128 v[220:223], %[ax1]\n\t"
      "ds_read_b64_tr_b16 v[232:233], %[aw1] offset:3072\n\t"
      "v_mfma_f32_32x32x16_bf16 %[a02], v[216:219], v[200:203], %[a02]\n\t"
      "ds_read_b64_tr_b16 v[234:235], %[aw1] offset:3840\n\t"
      "ds_read_b64_tr_b16 v[236:237], %[aw2] offset:3072\n\t"
      "v_mfma_f32_32x32x16_bf16 %[a10], v[208:211], v[204:207], %[a10]\n\t"
      "ds_read_b64_tr_b16 v[238:239], %[aw2] offset:3840\n\t"
      "ds_read_b128 v[224:227], %[ax1] offset:4096\n\t"
      "v_mfma_f32_32x32x16_bf16 %[a11], v[212:215], v[204:207], %[a11]\n\t"
      "v_mfma_f32_32x32x16_bf16 %[a12], v[216:219], v[204:207], %[a12]\n\t"
      "s_waitcnt lgkmcnt(0)\n\t"
      "v_mfma_f32_32x32x16_bf16 %[a00], v[228:231], v[220:223], %[a00]\n\t"
      "ds_read_b64_tr_b16 v[208:209], %[aw0] offset:6144\n\t"
      "ds_read_b64_tr_b16 v[210:211], %[aw0] offset:6912\n\t"
      "v_mfma_f32_32x32x16_bf16 %[a01], v[232:235], v[220:223], %[a01]\n\t"
      "ds_read_b128 v[200:203], %[ax2]\n\t"
      "ds_read_b64_tr_b16 v[212:213], %[aw1] offset:6144\n\t"
      "v_mfma_f32_32x32x16_bf16 %[a02], v[236:239], v[220:223], %[a02]\n\t"
      "ds_read_b64_tr_b16 v[214:215], %[aw1] offset:6912\n\t"
      "ds_read_b64_tr_b16 v[216:217], %[aw2] offset:6144\n\t"
      "v_mfma_f32_32x32x16_bf16 %[a10], v[228:231], v[224:227], %[a10]\n\t"
      "ds_read_b64_tr_b16 v[218:219], %[aw2] offset:6912\n\t"
      "ds_read_b128 v[204:207], %[ax2] offset:4096\n\t"
      "v_mfma_f32_32x32x16_bf16 %[a11], v[232:235], v[224:227], %[a11]\n\t"
      "v_mfma_f32_32x32x16_bf16 %[a12], v[236:239], v[224:227], %[a12]\n\t"
      "s_and_b32 %[d], %[cnt], 3\n\t"
      "s_cmp_eq_u32 %[d], 3\n\t"
      "s_cselect_b32 %[d], %[dneg], %[dpos]\n\t"
      "s_waitcnt lgkmcnt(0)\n\t"
      "v_mfma_f32_32x32x16_bf16 %[a00], v[208:211], v[200:203], %[a00]\n\t"
      "ds_read_b64_tr_b16 v[228:229], %[aw0] offset:9216\n\t"
      "ds_read_b64_tr_b16 v[230:231], %[aw0] offset:9984\n\t"
      "v_mfma_f32_32x32x16_bf16 %[a01], v[212:215], v[200:203], %[a01]\n\t"
      "ds_read_b128 v[220:223], %[ax3]\n\t"
      "ds_read_b64_tr_b16 v[232:233], %[aw1] offset:9216\n\t"
      "v_mfma_f32_32x32x16_bf16 %[a02], v[216:219], v[200:203], %[a02]\n\t"
      "ds_read_b64_tr_b16 v[234:235], %[aw1] offset:9984\n\t"
      "ds_read_b64_tr_b16 v[236:237], %[aw2] offset:9216\n\t"
      "v_mfma_f32_32x32x16_bf16 %[a10], v[208:211], v[204:207], %[a10]\n\t"
      "ds_read_b64_tr_b16 v[238:239], %[aw2] offset:9984\n\t"
      "ds_read_b128 v[224:227], %[ax3] offset:4096\n\t"
      "v_mfma_f32_32x32x16_bf16 %[a11], v[212:215], v[204:207], %[a11]\n\t"
      "v_mfma_f32_32x32x16_bf16 %[a12], v[216:219], v[204:207], %[a12]\n\t"
      "s_waitcnt lgkmcnt(0)\n\t"
      "v_mfma_f32_32x32x16_bf16 %[a00], v[228:231], v[220:223], %[a00]\n\t"
      "v_add_u32 %[ax0], %[d], %[ax0]\n\t"
      "v_add_u32 %[aw0], %[d], %[aw0]\n\t"
      "v_add_u32 %[aw1], %[d], %[aw1]\n\t"
      "v_add_u32 %[aw2], %[d], %[aw2]\n\t"
      "v_mfma_f32_32x32x16_bf16 %[a01], v[232:235], v[220:223], %[a01]\n\t"
      "v_add_u32 %[ax1], %[d], %[ax1]\n\t"
      "v_add_u32 %[ax2], %[d], %[ax2]\n\t"
      "v_add_u32 %[ax3], %[d], %[ax3]\n\t"
      "s_add_u32 %[cnt], %[cnt], 1\n\t"
      "v_mfma_f32_32x32x16_bf16 %[a02], v[236:239], v[220:223], %[a02]\n\t"
      "s_barrier\n\t"
      "ds_read_b64_tr_b16 v[208:209], %[aw0] offset:0\n\t"
      "ds_read_b64_tr_b16 v[210:211], %[aw0] offset:768\n\t"
      "ds_read_b128 v[200:203], %[ax0]\n\t"
      "ds_read_b64_tr_b16 v[212:213], %[aw1] offset:0\n\t"
      "v_mfma_f32_32x32x16_bf16 %[a10], v[228:231], v[224:227], %[a10]\n\t"
      "ds_read_b64_tr_b16 v[214:215], %[aw1] offset:768\n\t"
      "ds_read_b64_tr_b16 v[216:217], %[aw2] offset:0\n\t"
      "v_mfma_f32_32x32x16_bf16 %[a11], v[232:235], v[224:227], %[a11]\n\t"
      "ds_read_b64_tr_b16 v[218:219], %[aw2] offset:768\n\t"
      "ds_read_b128 v[204:207], %[ax0] offset:4096\n\t"
      "v_mfma_f32_32x32x16_bf16 %[a12], v[236:239], v[224:227], %[a12]\n\t"
      "s_cmp_lt_u32 %[cnt], 8\n\t"
      "s_cbranch_scc1 1b\n\t"
      "s_waitcnt lgkmcnt(0)\n\t"
      "s_nop 15\n\t"
      : [a00] "+v"(a00), [a01] "+v"(a01), [a02] "+v"(a02), [a10] "+v"(a10), [a11] "+v"(a11), [a12] "+v"(a12),
        [ax0] "+v"(ax0), [ax1] "+v"(ax1), [ax2] "+v"(ax2), [ax3] "+v"(ax3), [aw0] "+v"(aw0), [aw1] "+v"(aw1), [aw2] "+v"(aw2),
        [cnt] "=&s"(cnt), [d] "=&s"(dd)
      : [dneg] "s"((u32)(-3 * SLOT)), [dpos] "s"((u32)SLOT)
      : "memory", "scc", "v200", "v201", "v202", "v203", "v204", "v205", "v206", "v207", "v208", "v209", "v210", "v211", "v212", "v213", "v214", "v215", "v216", "v217", "v218", "v219", "v220", "v221", "v222", "v223", "v224", "v225", "v226", "v227", "v228", "v229", "v230", "v231", "v232", "v233", "v234", "v235", "v236", "v237", "v238", "v239");
    STAMP(10);
    u32x4* hp = hfrag + ((size_t)(mi * 4 + wm * 2) * NKS + nj * 6 + wn * 3) * 64 + lane;
    const int nvalid = NKS - (nj * 6 + wn * 3);
#define EPI(acc, mb, t) do { if (t < nvalid) { u32x4 o; \
      _Pragma("unroll") for (int j = 0; j < 4; ++j) \
        o[j] = pack_bf16(geglu1(acc[2 * j], acc[8 + 2 * j]), geglu1(acc[2 * j + 1], acc[9 + 2 * j])); \
      hp[(mb * NKS + t) * 64] = o; } } while (0)
    EPI(a00, 0, 0); EPI(a01, 0, 1); EPI(a02, 0, 2); EPI(a10, 1, 0); EPI(a11, 1, 1); EPI(a12, 1, 2);
    STAMP(11);
  }
}

__global__ __launch_bounds__(512, 2) void k2(const u32x4* __restrict__ hfrag, const u16* __restrict__ wd, u16* __restrict__ out, u64* __restrict__ diag) {
  extern __shared__ __attribute__((aligned(16))) char smem[];
  const int lane = threadIdx.x & 63;
  const int wid = __builtin_amdgcn_readfirstlane(threadIdx.x >> 6);
  const int b = blockIdx.x, g = b & 7, r = b >> 3;
  const int mp = g * 4 + (r & 3), nt = r >> 2;
  const int kq = wid >> 1, mb = wid & 1;
  const u32 lds0 = (u32)(size_t)smem;
  STAMP(16);
  const int ks0 = kq * 22;
  __amdgpu_buffer_rsrc_t rd = __builtin_amdgcn_make_buffer_rsrc((void*)wd, 0, DF * DM * 2, 0x00020000);
  __amdgpu_buffer_rsrc_t rh = __builtin_amdgcn_make_buffer_rsrc((void*)hfrag, 0, 64 * NKS * 1024, 0x00020000);
  const u32 vd = (u32)((lane >> 3) * 1024 + (((lane & 7) ^ (((lane >> 4) & 1) << 2)) << 4));
  const u32 sd0 = (u32)((ks0 * 16 + mb * 32) * 1024 + nt * 128);
  const u32 vh = (u32)(lane * 16);
  const u32 sh0 = (u32)(((mp * 2 + mb) * NKS + ks0) * 1024);
  const u32 shl = (kq == 3) ? 0x7ffff000u : sh0 + 20 * 1024;
  u32x4 hf[22];
#define HLOAD(i, so, off) asm volatile("buffer_load_dwordx4 %0, %1, %2, %3 offen offset:%4" : "=v"(hf[i]) : "v"(vh), "s"(rh), "s"(so), "i"(off) : "memory")
#pragma unroll
  for (int st = 0; st < 5; ++st) {
    const u32 base = lds0 + (u32)st * 32768 + kq * 8192 + mb * 4096;
#pragma unroll
    for (int i = 0; i < 4; ++i) DMA16(rd, base + i * 1024, vd, sd0 + (u32)((st * 64 + i * 8) * 1024));
    const u32 so = sh0 + st * 4096;
    HLOAD(st * 4 + 0, so, 0); HLOAD(st * 4 + 1, so, 1024); HLOAD(st * 4 + 2, so, 2048); HLOAD(st * 4 + 3, so, 3072);
  }
  HLOAD(20, shl, 0); HLOAD(21, shl, 1024);
  const int hi = lane >> 5, half = (lane >> 4) & 1, q = (lane >> 2) & 3, p = lane & 3;
  u32 ad0 = lds0 + (u32)(kq * 8192 + (8 * hi + q) * 128 + (((q >> 1) & 1) * 64) + half * 32 + p * 8);
  u32 ad1 = ad0 ^ 64;
  f32x16 acc0 = {}, acc1 = {};
  u32x2 w[4][4];
#define RDK(k, so) do { RDTR(w[k][0], ad0, (so) + (k) * 2048); RDTR(w[k][1], ad0, (so) + (k) * 2048 + 512); \
                        RDTR(w[k][2], ad1, (so) + (k) * 2048); RDTR(w[k][3], ad1, (so) + (k) * 2048 + 512); } while (0)
#define MMK(k, hidx) do { MFMA(acc0, CAT4(w[k][0], w[k][1]), hf[hidx]); MFMA(acc1, CAT4(w[k][2], w[k][3]), hf[hidx]); } while (0)
#define STAGE4(st, so) do { RDK(0, so); RDK(1, so); RDK(2, so); WAIT_L(8); MMK(0, st * 4); RDK(3, so); WAIT_L(8); MMK(1, st * 4 + 1); \
                            WAIT_L(4); MMK(2, st * 4 + 2); WAIT_L(0); MMK(3, st * 4 + 3); } while (0)
  WAIT_V(34); BAR(); STAMP(17); STAGE4(0, 0);
  WAIT_V(26); BAR(); STAMP(18);
#pragma unroll
  for (int i = 0; i < 2; ++i) DMA16(rd, lds0 + kq * 8192 + mb * 2048 + i * 1024, vd, (u32)((ks0 * 16 + 320 + mb * 16 + i * 8) * 1024 + nt * 128));
  STAGE4(1, 32768);
  ad0 += 65536; ad1 += 65536;
  WAIT_V(20); BAR(); STAMP(19); STAGE4(2, 0);
  WAIT_V(12); BAR(); STAMP(20); STAGE4(3, 32768);
  ad0 += 65536; ad1 += 65536;
  WAIT_V(4); BAR(); STAMP(21); STAGE4(4, 0);
  ad0 -= 131072; ad1 -= 131072;
  WAIT_V(0); BAR(); STAMP(22);
  RDK(0, 0); RDK(1, 0); WAIT_L(4); MMK(0, 20); WAIT_L(0); MMK(1, 21);
  asm volatile("s_nop 15\n\ts_nop 3" : "+v"(acc0), "+v"(acc1));
  {
    const u32 wb = lds0 + (u32)(32768 + mb * 32768 + kq * 8192 + lane * 16);
#define PART(p_, h_) f32x4{((p_) >> 1 ? acc1 : acc0)[((p_) & 1) * 8 + (h_) * 4], ((p_) >> 1 ? acc1 : acc0)[((p_) & 1) * 8 + (h_) * 4 + 1], \
                           ((p_) >> 1 ? acc1 : acc0)[((p_) & 1) * 8 + (h_) * 4 + 2], ((p_) >> 1 ? acc1 : acc0)[((p_) & 1) * 8 + (h_) * 4 + 3]}
#pragma unroll
    for (int part = 0; part < 4; ++part) {
      *(lds_f32x4p)(size_t)(wb + part * 2048) = PART(part, 0);
      *(lds_f32x4p)(size_t)(wb + part * 2048 + 1024) = PART(part, 1);
    }
    WAIT_L(0);
    BAR();
    STAMP(23);
    const u32 rb = lds0 + (u32)(32768 + mb * 32768 + kq * 2048 + lane * 16);
    f32x4 s0 = *(lds_f32x4p)(size_t)(rb), s1 = *(lds_f32x4p)(size_t)(rb + 1024);
#pragma unroll
    for (int src = 1; src < 4; ++src) {
      s0 += *(lds_f32x4p)(size_t)(rb + src * 8192);
      s1 += *(lds_f32x4p)(size_t)(rb + src * 8192 + 1024);
    }
    u32 a0 = pack_bf16(s0[0], s0[1]), a1 = pack_bf16(s0[2], s0[3]);
    u32 b0 = pack_bf16(s1[0], s1[1]), b1 = pack_bf16(s1[2], s1[3]);
    auto r0 = __builtin_amdgcn_permlane32_swap(a0, b0, false, false);
    auto r1 = __builtin_amdgcn_permlane32_swap(a1, b1, false, false);
    u32x4 o = {r0[0], r1[0], r0[1], r1[1]};
    const int row = mp * 64 + mb * 32 + (lane & 31);
    *(u32x4*)(out + (size_t)row * DM + nt * 64 + kq * 16 + hi * 8) = o;
    STAMP(24);
  }
}

extern "C" void kernel_launch(void* const* d_in, const int* in_sizes, int n_in, void* d_out, int out_size, void* d_ws,
                              size_t ws_size, hipStream_t stream) {
  const u16* x = (const u16*)d_in[0];
  const u16* wg = (const u16*)d_in[1];
  const u16* wu = (const u16*)d_in[2];
  const u16* wd = (const u16*)d_in[3];
  u64* diag = (u64*)((char*)d_ws + (64u << 20));
  (void)hipFuncSetAttribute((const void*)k1, hipFuncAttributeMaxDynamicSharedMemorySize, 163840);
  (void)hipFuncSetAttribute((const void*)k2, hipFuncAttributeMaxDynamicSharedMemorySize, 163840);
  k1<<<256, 512, 163840, stream>>>(x, wg, wu, (u32x4*)d_ws, diag);
  k2<<<256, 512, 163840, stream>>>((const u32x4*)d_ws, wd, (u16*)d_out, diag);
}
```

```cpp
#include <hip/hip_runtime.h>

__global__ __launch_bounds__(512, 2) void geglu_fused(const void* x, const void* wg, const void* wu, const void* wd, void* out, void* ws) {
  asm volatile(
      "s_load_dwordx8 s[8:15], %[ka], 0\n\t"
      "s_load_dwordx4 s[16:19], %[ka], 32\n\t"
      "v_mov_b32 v0, %[tid]\n\t"
      "s_mov_b32 s20, %[bid]\n\t"
      "v_lshrrev_b32 v3, 6, v0\n\t"
      "v_and_b32 v1, 63, v0\n\t"
      "s_and_b32 s22, s20, 7\n\t"
      "v_readfirstlane_b32 s21, v3\n\t"
      "s_lshr_b32 s23, s20, 3\n\t"
      "s_cmp_lt_u32 s23, 4\n\t"
      "s_cselect_b32 s44, 7, 99\n\t"
      "s_cmp_eq_u32 s21, s44\n\t"
      "s_cbranch_scc0 Lncw_%=\n\t"
      "s_getpc_b64 s[44:45]\n\t"
      "v_lshlrev_b32 v4, 7, v1\n\t"
      "v_add_u32 v5, 8192, v4\n\t"
      "v_min_u32 v5, Lend_%=-Lncw_%=, v5\n\t"
      "global_load_dword v248, v4, s[44:45]\n\t"
      "global_load_dword v249, v5, s[44:45]\n\t"
      "Lncw_%=:\n\t"
      "s_lshr_b32 s38, s23, 2\n\t"
      "s_and_b32 s44, s23, 3\n\t"
      "s_lshr_b32 s45, s22, 1\n\t"
      "s_lshl2_add_u32 s36, s45, s44\n\t"
      "s_and_b32 s39, s22, 1\n\t"
      "s_mul_i32 s37, s39, 7\n\t"
      "s_add_u32 s37, s37, s38\n\t"
      "s_add_u32 s46, s39, 14\n\t"
      "s_cmp_eq_u32 s38, 7\n\t"
      "s_cselect_b32 s37, s46, s37\n\t"
      "s_lshl1_add_u32 s41, s39, s45\n\t"
      "s_and_b32 s41, s41, 3\n\t"
      "s_add_u32 s46, s44, s38\n\t"
      "s_lshl2_add_u32 s41, s46, s41\n\t"
      "s_lshl1_add_u32 s42, s44, s22\n\t"
      "s_and_b32 s43, s21, 3\n\t"
      "s_cmp_lt_u32 s21, 4\n\t"
      "s_cbranch_scc1 Lcons_%=\n\t"
      "s_cmp_eq_u32 s21, 7\n\t"
      "s_cbranch_scc1 Lhelp_%=\n\t"
      "v_lshrrev_b32 v3, 3, v1\n\t"
      "s_cmp_eq_u32 s21, 4\n\t"
      "s_cselect_b32 s55, 1, 0\n\t"
      "s_cbranch_scc0 Lldw_%=\n\t"
      "v_lshrrev_b32 v4, 4, v1\n\t"
      "v_and_b32 v5, 7, v1\n\t"
      "v_xor_b32 v4, v4, v5\n\t"
      "v_lshlrev_b32 v4, 4, v4\n\t"
      "v_lshl_add_u32 v4, v3, 10, v4\n\t"
      "v_xor_b32 v5, 64, v4\n\t"
      "v_add_u32 v16, 0, v4\n\t"
      "v_add_u32 v17, 7168, v5\n\t"
      "v_add_u32 v18, 14336, v4\n\t"
      "v_add_u32 v19, 21504, v5\n\t"
      "v_add_u32 v20, 32768, v4\n\t"
      "v_add_u32 v21, 39936, v5\n\t"
      "v_add_u32 v22, 47104, v4\n\t"
      "v_add_u32 v23, 54272, v5\n\t"
      "v_add_u32 v24, 65536, v4\n\t"
      "v_add_u32 v25, 72704, v5\n\t"
      "v_add_u32 v26, 79872, v4\n\t"
      "v_add_u32 v27, 87040, v5\n\t"
      "v_add_u32 v28, 98304, v4\n\t"
      "v_add_u32 v29, 105472, v5\n\t"
      "v_add_u32 v30, 112640, v4\n\t"
      "v_add_u32 v31, 119808, v5\n\t"
      "s_lshl_b32 s52, s36, 17\n\t"
      "s_movk_i32 s53, 128\n\t"
      "s_mov_b32 s54, 0\n\t"
      "s_mov_b32 s26, 0x200000\n\t"
      "s_waitcnt lgkmcnt(0)\n\t"
      "s_mov_b32 s24, s8\n\t"
      "s_mov_b32 s25, s9\n\t"
      "s_branch Lldgo_%=\n\t"
      "Lldw_%=:\n\t"
      "s_sub_u32 s46, s21, 5\n\t"
      "s_lshl_b32 s47, s46, 1\n\t"
      "s_cmp_eq_u32 s37, 14\n\t"
      "s_cselect_b32 s48, 4, 12\n\t"
      "v_mov_b32 v10, 0x7f000000\n\t"
      "v_add_u32 v4, 0, v1\n\t"
      "v_mul_u32_u24 v5, 171, v4\n\t"
      "v_lshrrev_b32 v5, 11, v5\n\t"
      "v_mul_u32_u24 v6, 12, v5\n\t"
      "v_sub_u32 v4, v4, v6\n\t"
      "v_lshrrev_b32 v6, 2, v5\n\t"
      "v_and_b32 v6, 2, v6\n\t"
      "v_xor_b32 v4, v6, v4\n\t"
      "v_cmp_le_u32 vcc, s48, v4\n\t"
      "v_lshlrev_b32 v4, 4, v4\n\t"
      "v_mul_u32_u24 v5, 2752, v5\n\t"
      "v_add_u32 v7, v4, v5\n\t"
      "v_cndmask_b32 v7, v7, v10, vcc\n\t"
      "v_add_u32 v4, 64, v1\n\t"
      "v_mul_u32_u24 v5, 171, v4\n\t"
      "v_lshrrev_b32 v5, 11, v5\n\t"
      "v_mul_u32_u24 v6, 12, v5\n\t"
      "v_sub_u32 v4, v4, v6\n\t"
      "v_lshrrev_b32 v6, 2, v5\n\t"
      "v_and_b32 v6, 2, v6\n\t"
      "v_xor_b32 v4, v6, v4\n\t"
      "v_cmp_le_u32 vcc, s48, v4\n\t"
      "v_lshlrev_b32 v4, 4, v4\n\t"
      "v_mul_u32_u24 v5, 2752, v5\n\t"
      "v_add_u32 v8, v4, v5\n\t"
      "v_cndmask_b32 v8, v8, v10, vcc\n\t"
      "v_add_u32 v4, 128, v1\n\t"
      "v_mul_u32_u24 v5, 171, v4\n\t"
      "v_lshrrev_b32 v5, 11, v5\n\t"
      "v_mul_u32_u24 v6, 12, v5\n\t"
      "v_sub_u32 v4, v4, v6\n\t"
      "v_lshrrev_b32 v6, 2, v5\n\t"
      "v_and_b32 v6, 2, v6\n\t"
      "v_xor_b32 v4, v6, v4\n\t"
      "v_cmp_le_u32 vcc, s48, v4\n\t"
      "v_lshlrev_b32 v4, 4, v4\n\t"
      "v_mul_u32_u24 v5, 2752, v5\n\t"
      "v_add_u32 v9, v4, v5\n\t"
      "v_cndmask_b32 v9, v9, v10, vcc\n\t"
      "v_add_u32 v16, 0, v7\n\t"
      "v_add_u32 v17, -1024, v8\n\t"
      "v_add_u32 v18, -2048, v9\n\t"
      "v_add_u32 v19, 40960, v7\n\t"
      "v_add_u32 v20, 44032, v8\n\t"
      "v_add_u32 v21, 43008, v9\n\t"
      "v_add_u32 v22, 86016, v7\n\t"
      "v_add_u32 v23, 84992, v8\n\t"
      "v_add_u32 v24, 88064, v9\n\t"
      "v_add_u32 v25, 131072, v7\n\t"
      "v_add_u32 v26, 130048, v8\n\t"
      "v_add_u32 v27, 129024, v9\n\t"
      "s_mul_i32 s52, s37, 192\n\t"
      "s_mov_b32 s53, 176128\n\t"
      "s_mul_i32 s54, s46, 12288\n\t"
      "s_add_u32 s54, s54, 16384\n\t"
      "s_mov_b32 s26, 1409024\n\t"
      "s_cmp_eq_u32 s46, 0\n\t"
      "s_waitcnt lgkmcnt(0)\n\t"
      "s_cselect_b32 s24, s10, s12\n\t"
      "s_cselect_b32 s25, s11, s13\n\t"
      "Lldgo_%=:\n\t"
      "s_and_b32 s25, s25, 0xffff\n\t"
      "s_mov_b32 s27, 0x20000\n\t"
      "s_sub_u32 s44, s37, s55\n\t"
      "s_cmp_eq_u32 s44, 15\n\t"
      "s_cselect_b32 s26, 0, s26\n\t"
      "s_cmp_ge_u32 s37, 14\n\t"
      "s_cselect_b32 s44, 1, 0\n\t"
      "s_add_u32 s41, s41, s44\n\t"
      "s_mov_b32 s50, 0\n\t"
      "s_mov_b32 s51, s54\n\t"
      "Lldloop_%=:\n\t"
      "s_add_u32 s44, s50, s41\n\t"
      "s_and_b32 s44, s44, 7\n\t"
      "s_mul_i32 s44, s44, s53\n\t"
      "s_mov_b32 m0, s51\n\t"
      "s_add_u32 s44, s44, s52\n\t"
      "buffer_load_dwordx4 v16, s[24:27], s44 offen offset:0 lds\n\t"
      "buffer_load_dwordx4 v17, s[24:27], s44 offen offset:1024 lds\n\t"
      "buffer_load_dwordx4 v18, s[24:27], s44 offen offset:2048 lds\n\t"
      "buffer_load_dwordx4 v19, s[24:27], s44 offen offset:3072 lds\n\t"
      "s_add_u32 m0, s51, 4096\n\t"
      "s_nop 0\n\t"
      "buffer_load_dwordx4 v20, s[24:27], s44 offen offset:0 lds\n\t"
      "buffer_load_dwordx4 v21, s[24:27], s44 offen offset:1024 lds\n\t"
      "buffer_load_dwordx4 v22, s[24:27], s44 offen offset:2048 lds\n\t"
      "buffer_load_dwordx4 v23, s[24:27], s44 offen offset:3072 lds\n\t"
      "s_add_u32 m0, s51, 8192\n\t"
      "s_nop 0\n\t"
      "buffer_load_dwordx4 v24, s[24:27], s44 offen offset:0 lds\n\t"
      "buffer_load_dwordx4 v25, s[24:27], s44 offen offset:1024 lds\n\t"
      "buffer_load_dwordx4 v26, s[24:27], s44 offen offset:2048 lds\n\t"
      "buffer_load_dwordx4 v27, s[24:27], s44 offen offset:3072 lds\n\t"
      "s_add_u32 m0, s51, 12288\n\t"
      "s_nop 0\n\t"
      "s_cmp_eq_u32 s55, 0\n\t"
      "s_cbranch_scc1 Lldn4_%=\n\t"
      "buffer_load_dwordx4 v28, s[24:27], s44 offen offset:0 lds\n\t"
      "buffer_load_dwordx4 v29, s[24:27], s44 offen offset:1024 lds\n\t"
      "buffer_load_dwordx4 v30, s[24:27], s44 offen offset:2048 lds\n\t"
      "buffer_load_dwordx4 v31, s[24:27], s44 offen offset:3072 lds\n\t"
      "Lldn4_%=:\n\t"
      "s_add_u32 s51, s51, 40960\n\t"
      "s_cmp_ge_u32 s51, 122880\n\t"
      "s_cselect_b32 s51, s54, s51\n\t"
      "s_add_u32 s50, s50, 1\n\t"
      "s_cmp_eq_u32 s50, 1\n\t"
      "s_cbranch_scc1 Lldloop_%=\n\t"
      "s_cmp_eq_u32 s50, 2\n\t"
      "s_cbranch_scc0 Lldns_%=\n\t"
      "s_call_b64 s[98:99], Lsetup_%=\n\t"
      "s_call_b64 s[98:99], Lsetupb_%=\n\t"
      "Lldns_%=:\n\t"
      "s_cmp_eq_u32 s50, 8\n\t"
      "s_cbranch_scc0 Lldnc_%=\n\t"
      "ds_read_b64_tr_b16 v[32:33], v9 offset:0\n\t"
      "ds_read_b64_tr_b16 v[34:35], v9 offset:512\n\t"
      "ds_read_b64_tr_b16 v[36:37], v10 offset:0\n\t"
      "ds_read_b64_tr_b16 v[38:39], v10 offset:512\n\t"
      "ds_read_b64_tr_b16 v[40:41], v9 offset:2048\n\t"
      "ds_read_b64_tr_b16 v[42:43], v9 offset:2560\n\t"
      "ds_read_b64_tr_b16 v[44:45], v10 offset:2048\n\t"
      "ds_read_b64_tr_b16 v[46:47], v10 offset:2560\n\t"
      "s_waitcnt lgkmcnt(0)\n\t"
      "Lldnc_%=:\n\t"
      "s_cmp_eq_u32 s55, 0\n\t"
      "s_cbranch_scc1 Lldw12_%=\n\t"
      "s_waitcnt vmcnt(16)\n\t"
      "s_branch Lldbar_%=\n\t"
      "Lldw12_%=:\n\t"
      "s_waitcnt vmcnt(12)\n\t"
      "Lldbar_%=:\n\t"
      "s_barrier\n\t"
      "s_cmp_lt_u32 s50, 8\n\t"
      "s_cbranch_scc1 Lldloop_%=\n\t"
      "Lwdtail_%=:\n\t"
      "s_add_u32 s44, s42, 0\n\t"
      "s_and_b32 s44, s44, 3\n\t"
      "s_mul_i32 s44, s44, 81920\n\t"
      "s_add_u32 s44, s44, s62\n\t"
      "s_mul_i32 s45, s43, 10240\n\t"
      "s_add_u32 s45, s45, 122880\n\t"
      "s_add_u32 m0, s45, 0\n\t"
      "s_nop 0\n\t"
      "buffer_load_dwordx4 v2, s[28:31], s44 offen offset:0 lds\n\t"
      "buffer_load_dwordx4 v11, s[28:31], s44 offen offset:1024 lds\n\t"
      "buffer_load_dwordx4 v12, s[28:31], s44 offen offset:2048 lds\n\t"
      "buffer_load_dwordx4 v13, s[28:31], s44 offen offset:3072 lds\n\t"
      "s_add_u32 m0, s45, 4096\n\t"
      "s_add_u32 s44, s44, 32768\n\t"
      "buffer_load_dwordx4 v2, s[28:31], s44 offen offset:0 lds\n\t"
      "buffer_load_dwordx4 v11, s[28:31], s44 offen offset:1024 lds\n\t"
      "buffer_load_dwordx4 v12, s[28:31], s44 offen offset:2048 lds\n\t"
      "buffer_load_dwordx4 v13, s[28:31], s44 offen offset:3072 lds\n\t"
      "s_add_u32 m0, s45, 8192\n\t"
      "s_add_u32 s44, s44, 32768\n\t"
      "buffer_load_dwordx4 v2, s[28:31], s44 offen offset:0 lds\n\t"
      "buffer_load_dwordx4 v11, s[28:31], s44 offen offset:1024 lds\n\t"
      "s_waitcnt vmcnt(10)\n\t"
      "s_barrier\n\t"
      "s_mov_b32 exec_lo, s35\n\t"
      "s_mov_b32 exec_hi, 0\n\t"
      "global_load_dwordx2 v[52:53], v253, s[18:19] sc1\n\t"
      "global_load_dword v254, v255, s[18:19] sc1\n\t"
      "s_mov_b64 exec, -1\n\t"
      "s_add_u32 s44, s42, 1\n\t"
      "s_and_b32 s44, s44, 3\n\t"
      "s_mul_i32 s44, s44, 81920\n\t"
      "s_add_u32 s44, s44, s62\n\t"
      "s_mul_i32 s45, s43, 10240\n\t"
      "s_add_u32 s45, s45, 81920\n\t"
      "s_add_u32 m0, s45, 0\n\t"
      "s_nop 0\n\t"
      "buffer_load_dwordx4 v2, s[28:31], s44 offen offset:0 lds\n\t"
      "buffer_load_dwordx4 v11, s[28:31], s44 offen offset:1024 lds\n\t"
      "buffer_load_dwordx4 v12, s[28:31], s44 offen offset:2048 lds\n\t"
      "buffer_load_dwordx4 v13, s[28:31], s44 offen offset:3072 lds\n\t"
      "s_add_u32 m0, s45, 4096\n\t"
      "s_add_u32 s44, s44, 32768\n\t"
      "buffer_load_dwordx4 v2, s[28:31], s44 offen offset:0 lds\n\t"
      "buffer_load_dwordx4 v11, s[28:31], s44 offen offset:1024 lds\n\t"
      "buffer_load_dwordx4 v12, s[28:31], s44 offen offset:2048 lds\n\t"
      "buffer_load_dwordx4 v13, s[28:31], s44 offen offset:3072 lds\n\t"
      "s_add_u32 m0, s45, 8192\n\t"
      "s_add_u32 s44, s44, 32768\n\t"
      "buffer_load_dwordx4 v2, s[28:31], s44 offen offset:0 lds\n\t"
      "buffer_load_dwordx4 v11, s[28:31], s44 offen offset:1024 lds\n\t"
      "s_add_u32 s44, s42, 2\n\t"
      "s_and_b32 s44, s44, 3\n\t"
      "s_mul_i32 s44, s44, 81920\n\t"
      "s_add_u32 s44, s44, s62\n\t"
      "s_mul_i32 s45, s43, 10240\n\t"
      "s_add_u32 s45, s45, 0\n\t"
      "s_add_u32 m0, s45, 0\n\t"
      "s_nop 0\n\t"
      "buffer_load_dwordx4 v2, s[28:31], s44 offen offset:0 lds\n\t"
      "buffer_load_dwordx4 v11, s[28:31], s44 offen offset:1024 lds\n\t"
      "buffer_load_dwordx4 v12, s[28:31], s44 offen offset:2048 lds\n\t"
      "buffer_load_dwordx4 v13, s[28:31], s44 offen offset:3072 lds\n\t"
      "s_call_b64 s[98:99], Lsetupc_%=\n\t"
      "s_waitcnt vmcnt(14)\n\t"
      "s_cmp_eq_u32 s21, 7\n\t"
      "s_cbranch_scc1 Lhchk_%=\n\t"
      "Labar8_%=:\n\t"
      "s_call_b64 s[98:99], Lmval_%=\n\t"
      "s_barrier\n\t"
      "s_add_u32 s46, s63, 1\n\t"
      "v_mov_b32 v190, s46\n\t"
      "s_lshl_b64 exec, 1, s34\n\t"
      "global_atomic_add v190, v252, v193, s[18:19] sc0 sc1\n\t"
      "s_mov_b64 exec, -1\n\t"
      "s_add_u32 s44, s42, 2\n\t"
      "s_and_b32 s44, s44, 3\n\t"
      "s_mul_i32 s44, s44, 81920\n\t"
      "s_add_u32 s44, s44, s62\n\t"
      "s_mul_i32 s45, s43, 10240\n\t"
      "s_add_u32 s45, s45, 0\n\t"
      "s_add_u32 s44, s44, 0\n\t"
      "s_add_u32 m0, s45, 4096\n\t"
      "s_nop 0\n\t"
      "s_add_u32 m0, s45, 4096\n\t"
      "s_add_u32 s44, s44, 32768\n\t"
      "buffer_load_dwordx4 v2, s[28:31], s44 offen offset:0 lds\n\t"
      "buffer_load_dwordx4 v11, s[28:31], s44 offen offset:1024 lds\n\t"
      "buffer_load_dwordx4 v12, s[28:31], s44 offen offset:2048 lds\n\t"
      "buffer_load_dwordx4 v13, s[28:31], s44 offen offset:3072 lds\n\t"
      "s_add_u32 m0, s45, 8192\n\t"
      "s_add_u32 s44, s44, 32768\n\t"
      "buffer_load_dwordx4 v2, s[28:31], s44 offen offset:0 lds\n\t"
      "buffer_load_dwordx4 v11, s[28:31], s44 offen offset:1024 lds\n\t"
      "s_waitcnt vmcnt(6)\n\t"
      "Lp2_%=:\n\t"
      "v_readlane_b32 s46, v190, s34\n\t"
      "s_and_b32 s46, s46, 0xffffff\n\t"
      "s_cmp_eq_u32 s46, s63\n\t"
      "s_cbranch_scc0 Lslow_%=\n\t"
      "buffer_load_dwordx4 v[64:67], v170, s[4:7], s56 offen offset:0 sc1\n\t"
      "buffer_load_dwordx4 v[68:71], v170, s[4:7], s56 offen offset:1024 sc1\n\t"
      "buffer_load_dwordx4 v[72:75], v170, s[4:7], s56 offen offset:2048 sc1\n\t"
      "buffer_load_dwordx4 v[76:79], v170, s[4:7], s56 offen offset:3072 sc1\n\t"
      "buffer_load_dwordx4 v[80:83], v171, s[4:7], s56 offen offset:0 sc1\n\t"
      "buffer_load_dwordx4 v[84:87], v170, s[4:7], s57 offen offset:0 sc1\n\t"
      "s_waitcnt vmcnt(6)\n\t"
      "s_barrier\n\t"
      "Lstream_%=:\n\t"
      "ds_read_b64_tr_b16 v[152:153], v172 offset:0\n\t"
      "ds_read_b64_tr_b16 v[154:155], v172 offset:512\n\t"
      "ds_read_b64_tr_b16 v[156:157], v173 offset:0\n\t"
      "ds_read_b64_tr_b16 v[158:159], v173 offset:512\n\t"
      "ds_read_b64_tr_b16 v[160:161], v172 offset:2048\n\t"
      "ds_read_b64_tr_b16 v[162:163], v172 offset:2560\n\t"
      "ds_read_b64_tr_b16 v[164:165], v173 offset:2048\n\t"
      "ds_read_b64_tr_b16 v[166:167], v173 offset:2560\n\t"
      "s_cmp_ge_u32 s43, 2\n\t"
      "s_cselect_b64 vcc, -1, 0\n\t"
      "s_cmp_lt_u32 s21, 4\n\t"
      "s_waitcnt vmcnt(5)\n\t"
      "buffer_load_dwordx4 v[88:91], v170, s[4:7], s57 offen offset:1024 sc1\n\t"
      "s_waitcnt lgkmcnt(4)\n\t"
      "v_mfma_f32_32x32x16_bf16 v[196:211], v[152:155], v[64:67], v[196:211]\n\t"
      "v_mfma_f32_32x32x16_bf16 v[212:227], v[156:159], v[64:67], v[212:227]\n\t"
      "ds_read_b64_tr_b16 v[152:153], v172 offset:4096\n\t"
      "ds_read_b64_tr_b16 v[154:155], v172 offset:4608\n\t"
      "ds_read_b64_tr_b16 v[156:157], v173 offset:4096\n\t"
      "ds_read_b64_tr_b16 v[158:159], v173 offset:4608\n\t"
      "s_waitcnt vmcnt(5)\n\t"
      "buffer_load_dwordx4 v[92:95], v170, s[4:7], s57 offen offset:2048 sc1\n\t"
      "s_waitcnt lgkmcnt(4)\n\t"
      "v_mfma_f32_32x32x16_bf16 v[196:211], v[160:163], v[68:71], v[196:211]\n\t"
      "v_mfma_f32_32x32x16_bf16 v[212:227], v[164:167], v[68:71], v[212:227]\n\t"
      "ds_read_b64_tr_b16 v[160:161], v172 offset:6144\n\t"
      "ds_read_b64_tr_b16 v[162:163], v172 offset:6656\n\t"
      "ds_read_b64_tr_b16 v[164:165], v173 offset:6144\n\t"
      "ds_read_b64_tr_b16 v[166:167], v173 offset:6656\n\t"
      "s_waitcnt vmcnt(5)\n\t"
      "buffer_load_dwordx4 v[96:99], v170, s[4:7], s57 offen offset:3072 sc1\n\t"
      "s_waitcnt lgkmcnt(4)\n\t"
      "v_mfma_f32_32x32x16_bf16 v[196:211], v[152:155], v[72:75], v[196:211]\n\t"
      "v_mfma_f32_32x32x16_bf16 v[212:227], v[156:159], v[72:75], v[212:227]\n\t"
      "ds_read_b64_tr_b16 v[152:153], v172 offset:8192\n\t"
      "ds_read_b64_tr_b16 v[154:155], v172 offset:8704\n\t"
      "ds_read_b64_tr_b16 v[156:157], v173 offset:8192\n\t"
      "ds_read_b64_tr_b16 v[158:159], v173 offset:8704\n\t"
      "s_waitcnt vmcnt(5)\n\t"
      "buffer_load_dwordx4 v[100:103], v171, s[4:7], s57 offen offset:0 sc1\n\t"
      "s_waitcnt lgkmcnt(4)\n\t"
      "v_mfma_f32_32x32x16_bf16 v[196:211], v[160:163], v[76:79], v[196:211]\n\t"
      "v_mfma_f32_32x32x16_bf16 v[212:227], v[164:167], v[76:79], v[212:227]\n\t"
      "ds_read_b64_tr_b16 v[160:161], v174 offset:0\n\t"
      "ds_read_b64_tr_b16 v[162:163], v174 offset:512\n\t"
      "ds_read_b64_tr_b16 v[164:165], v175 offset:0\n\t"
      "ds_read_b64_tr_b16 v[166:167], v175 offset:512\n\t"
      "s_waitcnt vmcnt(5)\n\t"
      "buffer_load_dwordx4 v[104:107], v170, s[4:7], s58 offen offset:0 sc1\n\t"
      "s_waitcnt lgkmcnt(4)\n\t"
      "v_mfma_f32_32x32x16_bf16 v[196:211], v[152:155], v[80:83], v[196:211]\n\t"
      "v_mfma_f32_32x32x16_bf16 v[212:227], v[156:159], v[80:83], v[212:227]\n\t"
      "ds_read_b64_tr_b16 v[152:153], v174 offset:2048\n\t"
      "ds_read_b64_tr_b16 v[154:155], v174 offset:2560\n\t"
      "ds_read_b64_tr_b16 v[156:157], v175 offset:2048\n\t"
      "ds_read_b64_tr_b16 v[158:159], v175 offset:2560\n\t"
      "s_waitcnt vmcnt(5)\n\t"
      "buffer_load_dwordx4 v[108:111], v170, s[4:7], s58 offen offset:1024 sc1\n\t"
      "s_waitcnt lgkmcnt(4)\n\t"
      "v_mfma_f32_32x32x16_bf16 v[196:211], v[160:163], v[84:87], v[196:211]\n\t"
      "v_mfma_f32_32x32x16_bf16 v[212:227], v[164:167], v[84:87], v[212:227]\n\t"
      "ds_read_b64_tr_b16 v[160:161], v174 offset:4096\n\t"
      "ds_read_b64_tr_b16 v[162:163], v174 offset:4608\n\t"
      "ds_read_b64_tr_b16 v[164:165], v175 offset:4096\n\t"
      "ds_read_b64_tr_b16 v[166:167], v175 offset:4608\n\t"
      "s_waitcnt vmcnt(5)\n\t"
      "buffer_load_dwordx4 v[112:115], v170, s[4:7], s58 offen offset:2048 sc1\n\t"
      "s_waitcnt lgkmcnt(4)\n\t"
      "v_mfma_f32_32x32x16_bf16 v[196:211], v[152:155], v[88:91], v[196:211]\n\t"
      "v_mfma_f32_32x32x16_bf16 v[212:227], v[156:159], v[88:91], v[212:227]\n\t"
      "ds_read_b64_tr_b16 v[152:153], v174 offset:6144\n\t"
      "ds_read_b64_tr_b16 v[154:155], v174 offset:6656\n\t"
      "ds_read_b64_tr_b16 v[156:157], v175 offset:6144\n\t"
      "ds_read_b64_tr_b16 v[158:159], v175 offset:6656\n\t"
      "s_waitcnt vmcnt(5)\n\t"
      "buffer_load_dwordx4 v[116:119], v170, s[4:7], s58 offen offset:3072 sc1\n\t"
      "s_waitcnt lgkmcnt(4)\n\t"
      "v_mfma_f32_32x32x16_bf16 v[196:211], v[160:163], v[92:95], v[196:211]\n\t"
      "v_mfma_f32_32x32x16_bf16 v[212:227], v[164:167], v[92:95], v[212:227]\n\t"
      "ds_read_b64_tr_b16 v[160:161], v174 offset:8192\n\t"
      "ds_read_b64_tr_b16 v[162:163], v174 offset:8704\n\t"
      "ds_read_b64_tr_b16 v[164:165], v175 offset:8192\n\t"
      "ds_read_b64_tr_b16 v[166:167], v175 offset:8704\n\t"
      "s_waitcnt vmcnt(5)\n\t"
      "buffer_load_dwordx4 v[120:123], v171, s[4:7], s58 offen offset:0 sc1\n\t"
      "s_waitcnt lgkmcnt(4)\n\t"
      "v_mfma_f32_32x32x16_bf16 v[196:211], v[152:155], v[96:99], v[196:211]\n\t"
      "v_mfma_f32_32x32x16_bf16 v[212:227], v[156:159], v[96:99], v[212:227]\n\t"
      "ds_read_b64_tr_b16 v[152:153], v176 offset:0\n\t"
      "ds_read_b64_tr_b16 v[154:155], v176 offset:512\n\t"
      "ds_read_b64_tr_b16 v[156:157], v177 offset:0\n\t"
      "ds_read_b64_tr_b16 v[158:159], v177 offset:512\n\t"
      "s_waitcnt vmcnt(5)\n\t"
      "buffer_load_dwordx4 v[124:127], v170, s[4:7], s59 offen offset:0 sc1\n\t"
      "s_waitcnt lgkmcnt(4)\n\t"
      "v_mfma_f32_32x32x16_bf16 v[196:211], v[160:163], v[100:103], v[196:211]\n\t"
      "v_mfma_f32_32x32x16_bf16 v[212:227], v[164:167], v[100:103], v[212:227]\n\t"
      "ds_read_b64_tr_b16 v[160:161], v176 offset:2048\n\t"
      "ds_read_b64_tr_b16 v[162:163], v176 offset:2560\n\t"
      "ds_read_b64_tr_b16 v[164:165], v177 offset:2048\n\t"
      "ds_read_b64_tr_b16 v[166:167], v177 offset:2560\n\t"
      "s_waitcnt vmcnt(5)\n\t"
      "buffer_load_dwordx4 v[128:131], v170, s[4:7], s59 offen offset:1024 sc1\n\t"
      "s_waitcnt lgkmcnt(4)\n\t"
      "v_mfma_f32_32x32x16_bf16 v[196:211], v[152:155], v[104:107], v[196:211]\n\t"
      "v_mfma_f32_32x32x16_bf16 v[212:227], v[156:159], v[104:107], v[212:227]\n\t"
      "ds_read_b64_tr_b16 v[152:153], v176 offset:4096\n\t"
      "ds_read_b64_tr_b16 v[154:155], v176 offset:4608\n\t"
      "ds_read_b64_tr_b16 v[156:157], v177 offset:4096\n\t"
      "ds_read_b64_tr_b16 v[158:159], v177 offset:4608\n\t"
      "s_waitcnt vmcnt(5)\n\t"
      "buffer_load_dwordx4 v[132:135], v170, s[4:7], s59 offen offset:2048 sc1\n\t"
      "s_waitcnt lgkmcnt(4)\n\t"
      "v_mfma_f32_32x32x16_bf16 v[196:211], v[160:163], v[108:111], v[196:211]\n\t"
      "v_mfma_f32_32x32x16_bf16 v[212:227], v[164:167], v[108:111], v[212:227]\n\t"
      "ds_read_b64_tr_b16 v[160:161], v176 offset:6144\n\t"
      "ds_read_b64_tr_b16 v[162:163], v176 offset:6656\n\t"
      "ds_read_b64_tr_b16 v[164:165], v177 offset:6144\n\t"
      "ds_read_b64_tr_b16 v[166:167], v177 offset:6656\n\t"
      "s_waitcnt vmcnt(5)\n\t"
      "buffer_load_dwordx4 v[136:139], v170, s[4:7], s59 offen offset:3072 sc1\n\t"
      "s_waitcnt lgkmcnt(4)\n\t"
      "v_mfma_f32_32x32x16_bf16 v[196:211], v[152:155], v[112:115], v[196:211]\n\t"
      "v_mfma_f32_32x32x16_bf16 v[212:227], v[156:159], v[112:115], v[212:227]\n\t"
      "ds_read_b64_tr_b16 v[152:153], v176 offset:8192\n\t"
      "ds_read_b64_tr_b16 v[154:155], v176 offset:8704\n\t"
      "ds_read_b64_tr_b16 v[156:157], v177 offset:8192\n\t"
      "ds_read_b64_tr_b16 v[158:159], v177 offset:8704\n\t"
      "s_waitcnt vmcnt(5)\n\t"
      "buffer_load_dwordx4 v[140:143], v171, s[4:7], s59 offen offset:0 sc1\n\t"
      "s_waitcnt lgkmcnt(4)\n\t"
      "v_mfma_f32_32x32x16_bf16 v[196:211], v[160:163], v[116:119], v[196:211]\n\t"
      "v_mfma_f32_32x32x16_bf16 v[212:227], v[164:167], v[116:119], v[212:227]\n\t"
      "ds_read_b64_tr_b16 v[160:161], v178 offset:0\n\t"
      "ds_read_b64_tr_b16 v[162:163], v178 offset:512\n\t"
      "ds_read_b64_tr_b16 v[164:165], v179 offset:0\n\t"
      "ds_read_b64_tr_b16 v[166:167], v179 offset:512\n\t"
      "s_waitcnt vmcnt(5)\n\t"
      "buffer_load_dwordx4 v[144:147], v170, s[4:7], s60 offen sc1\n\t"
      "s_waitcnt lgkmcnt(4)\n\t"
      "v_mfma_f32_32x32x16_bf16 v[196:211], v[152:155], v[120:123], v[196:211]\n\t"
      "v_mfma_f32_32x32x16_bf16 v[212:227], v[156:159], v[120:123], v[212:227]\n\t"
      "ds_read_b64_tr_b16 v[152:153], v178 offset:2048\n\t"
      "ds_read_b64_tr_b16 v[154:155], v178 offset:2560\n\t"
      "ds_read_b64_tr_b16 v[156:157], v179 offset:2048\n\t"
      "ds_read_b64_tr_b16 v[158:159], v179 offset:2560\n\t"
      "s_waitcnt vmcnt(5)\n\t"
      "buffer_load_dwordx4 v[148:151], v170, s[4:7], s61 offen sc1\n\t"
      "s_waitcnt lgkmcnt(4)\n\t"
      "v_mfma_f32_32x32x16_bf16 v[196:211], v[160:163], v[124:127], v[196:211]\n\t"
      "v_mfma_f32_32x32x16_bf16 v[212:227], v[164:167], v[124:127], v[212:227]\n\t"
      "ds_read_b64_tr_b16 v[160:161], v178 offset:4096\n\t"
      "ds_read_b64_tr_b16 v[162:163], v178 offset:4608\n\t"
      "ds_read_b64_tr_b16 v[164:165], v179 offset:4096\n\t"
      "ds_read_b64_tr_b16 v[166:167], v179 offset:4608\n\t"
      "s_waitcnt vmcnt(5)\n\t"
      "s_waitcnt lgkmcnt(4)\n\t"
      "v_mfma_f32_32x32x16_bf16 v[196:211], v[152:155], v[128:131], v[196:211]\n\t"
      "v_mfma_f32_32x32x16_bf16 v[212:227], v[156:159], v[128:131], v[212:227]\n\t"
      "ds_read_b64_tr_b16 v[152:153], v178 offset:6144\n\t"
      "ds_read_b64_tr_b16 v[154:155], v178 offset:6656\n\t"
      "ds_read_b64_tr_b16 v[156:157], v179 offset:6144\n\t"
      "ds_read_b64_tr_b16 v[158:159], v179 offset:6656\n\t"
      "v_and_b32 v3, 31, v1\n\t"
      "v_lshrrev_b32 v4, 5, v1\n\t"
      "v_and_b32 v5, 7, v1\n\t"
      "v_xor_b32 v4, v4, v5\n\t"
      "v_lshlrev_b32 v4, 4, v4\n\t"
      "v_lshl_add_u32 v4, v3, 8, v4\n\t"
      "s_lshl_b32 s44, s21, 13\n\t"
      "v_add_u32 v228, s44, v4\n\t"
      "v_xor_b32 v229, 32, v228\n\t"
      "v_xor_b32 v230, 64, v228\n\t"
      "v_xor_b32 v231, 96, v228\n\t"
      "s_waitcnt vmcnt(4)\n\t"
      "s_waitcnt lgkmcnt(4)\n\t"
      "v_mfma_f32_32x32x16_bf16 v[196:211], v[160:163], v[132:135], v[196:211]\n\t"
      "v_mfma_f32_32x32x16_bf16 v[212:227], v[164:167], v[132:135], v[212:227]\n\t"
      "ds_read_b64_tr_b16 v[160:161], v178 offset:8192\n\t"
      "ds_read_b64_tr_b16 v[162:163], v178 offset:8704\n\t"
      "ds_read_b64_tr_b16 v[164:165], v179 offset:8192\n\t"
      "ds_read_b64_tr_b16 v[166:167], v179 offset:8704\n\t"
      "v_lshrrev_b32 v3, 3, v1\n\t"
      "v_lshlrev_b32 v4, 1, v5\n\t"
      "v_xor_b32 v4, v4, v3\n\t"
      "v_lshlrev_b32 v4, 4, v4\n\t"
      "v_lshl_add_u32 v4, v3, 8, v4\n\t"
      "s_lshr_b32 s44, s21, 2\n\t"
      "s_lshl_b32 s44, s44, 15\n\t"
      "s_lshl_b32 s45, s43, 11\n\t"
      "s_add_u32 s44, s44, s45\n\t"
      "v_add_u32 v232, s44, v4\n\t"
      "v_xor_b32 v233, 16, v232\n\t"
      "v_lshlrev_b32 v5, 4, v5\n\t"
      "v_lshl_add_u32 v234, v3, 10, v5\n\t"
      "s_xor_b32 s44, s21, 4\n\t"
      "s_lshl_b32 s44, s44, 13\n\t"
      "s_lshl_b32 s45, s40, 16\n\t"
      "s_add_u32 s44, s44, s45\n\t"
      "s_lshl_b32 s45, s38, 7\n\t"
      "s_add_u32 s44, s44, s45\n\t"
      "v_add_u32 v234, s44, v234\n\t"
      "s_cmp_lt_u32 s21, 4\n\t"
      "s_waitcnt vmcnt(3)\n\t"
      "s_waitcnt lgkmcnt(4)\n\t"
      "v_mfma_f32_32x32x16_bf16 v[196:211], v[152:155], v[136:139], v[196:211]\n\t"
      "v_mfma_f32_32x32x16_bf16 v[212:227], v[156:159], v[136:139], v[212:227]\n\t"
      "s_waitcnt vmcnt(2)\n\t"
      "s_waitcnt lgkmcnt(0)\n\t"
      "s_cbranch_scc0 Lnb10_%=\n\t"
      "s_barrier\n\t"
      "Lnb10_%=:\n\t"
      "v_mfma_f32_32x32x16_bf16 v[196:211], v[160:163], v[140:143], v[196:211]\n\t"
      "v_mfma_f32_32x32x16_bf16 v[212:227], v[164:167], v[140:143], v[212:227]\n\t"
      "s_waitcnt vmcnt(1)\n\t"
      "v_mfma_f32_32x32x16_bf16 v[196:211], v[32:35], v[144:147], v[196:211]\n\t"
      "v_mfma_f32_32x32x16_bf16 v[212:227], v[36:39], v[144:147], v[212:227]\n\t"
      "s_waitcnt vmcnt(0)\n\t"
      "s_cbranch_vccnz Lno21_%=\n\t"
      "v_mfma_f32_32x32x16_bf16 v[196:211], v[40:43], v[148:151], v[196:211]\n\t"
      "v_mfma_f32_32x32x16_bf16 v[212:227], v[44:47], v[148:151], v[212:227]\n\t"
      "Lno21_%=:\n\t"
      "s_cbranch_scc1 Lnb10a_%=\n\t"
      "s_barrier\n\t"
      "Lnb10a_%=:\n\t"
      "s_nop 7\n\t"
      "s_nop 4\n\t"
      "ds_write_b128 v228, v[196:199] offset:0\n\t"
      "ds_write_b128 v229, v[200:203] offset:0\n\t"
      "ds_write_b128 v230, v[204:207] offset:0\n\t"
      "ds_write_b128 v231, v[208:211] offset:0\n\t"
      "ds_write_b128 v228, v[212:215] offset:128\n\t"
      "ds_write_b128 v229, v[216:219] offset:128\n\t"
      "ds_write_b128 v230, v[220:223] offset:128\n\t"
      "ds_write_b128 v231, v[224:227] offset:128\n\t"
      "s_waitcnt lgkmcnt(0)\n\t"
      "s_barrier\n\t"
      "ds_read_b128 v[64:67], v232 offset:0\n\t"
      "ds_read_b128 v[68:71], v233 offset:0\n\t"
      "ds_read_b128 v[72:75], v232 offset:8192\n\t"
      "ds_read_b128 v[76:79], v233 offset:8192\n\t"
      "ds_read_b128 v[80:83], v232 offset:16384\n\t"
      "ds_read_b128 v[84:87], v233 offset:16384\n\t"
      "ds_read_b128 v[88:91], v232 offset:24576\n\t"
      "ds_read_b128 v[92:95], v233 offset:24576\n\t"
      "s_waitcnt lgkmcnt(4)\n\t"
      "v_pk_add_f32 v[64:65], v[64:65], v[72:73]\n\t"
      "v_pk_add_f32 v[66:67], v[66:67], v[74:75]\n\t"
      "v_pk_add_f32 v[68:69], v[68:69], v[76:77]\n\t"
      "v_pk_add_f32 v[70:71], v[70:71], v[78:79]\n\t"
      "s_waitcnt lgkmcnt(0)\n\t"
      "v_pk_add_f32 v[80:81], v[80:81], v[88:89]\n\t"
      "v_pk_add_f32 v[82:83], v[82:83], v[90:91]\n\t"
      "v_pk_add_f32 v[84:85], v[84:85], v[92:93]\n\t"
      "v_pk_add_f32 v[86:87], v[86:87], v[94:95]\n\t"
      "v_pk_add_f32 v[64:65], v[64:65], v[80:81]\n\t"
      "v_pk_add_f32 v[66:67], v[66:67], v[82:83]\n\t"
      "v_pk_add_f32 v[68:69], v[68:69], v[84:85]\n\t"
      "v_pk_add_f32 v[70:71], v[70:71], v[86:87]\n\t"
      "v_cvt_pk_bf16_f32 v4, v64, v65\n\t"
      "v_cvt_pk_bf16_f32 v5, v66, v67\n\t"
      "v_cvt_pk_bf16_f32 v6, v68, v69\n\t"
      "v_cvt_pk_bf16_f32 v7, v70, v71\n\t"
      "global_store_dwordx4 v234, v[4:7], s[16:17] sc1\n\t"
      "s_endpgm\n\t"
      "Lslow_%=:\n\t"
      "s_add_u32 s46, s63, 1\n\t"
      "v_mov_b32 v190, s46\n\t"
      "s_lshl_b64 exec, 1, s34\n\t"
      "global_atomic_add v190, v252, v193, s[18:19] sc0 sc1\n\t"
      "s_mov_b64 exec, -1\n\t"
      "s_waitcnt vmcnt(1)\n\t"
      "s_barrier\n\t"
      "s_mov_b32 s50, 0\n\t"
      "s_branch Lpchk_%=\n\t"
      "Lpoll_%=:\n\t"
      "s_add_u32 s46, s63, 1\n\t"
      "v_mov_b32 v190, s46\n\t"
      "s_lshl_b64 exec, 1, s34\n\t"
      "global_atomic_add v190, v252, v193, s[18:19] sc0 sc1\n\t"
      "s_mov_b64 exec, -1\n\t"
      "Lpchk_%=:\n\t"
      "s_waitcnt vmcnt(0)\n\t"
      "v_readlane_b32 s46, v190, s34\n\t"
      "s_and_b32 s46, s46, 0xffffff\n\t"
      "s_cmp_eq_u32 s46, s63\n\t"
      "s_cbranch_scc1 Lgo_%=\n\t"
      "s_add_u32 s50, s50, 1\n\t"
      "s_cmp_gt_u32 s50, 200000\n\t"
      "s_cbranch_scc1 Lgo_%=\n\t"
      "s_sleep 1\n\t"
      "s_branch Lpoll_%=\n\t"
      "Lgo_%=:\n\t"
      "buffer_load_dwordx4 v[64:67], v170, s[4:7], s56 offen offset:0 sc1\n\t"
      "buffer_load_dwordx4 v[68:71], v170, s[4:7], s56 offen offset:1024 sc1\n\t"
      "buffer_load_dwordx4 v[72:75], v170, s[4:7], s56 offen offset:2048 sc1\n\t"
      "buffer_load_dwordx4 v[76:79], v170, s[4:7], s56 offen offset:3072 sc1\n\t"
      "buffer_load_dwordx4 v[80:83], v171, s[4:7], s56 offen offset:0 sc1\n\t"
      "buffer_load_dwordx4 v[84:87], v170, s[4:7], s57 offen offset:0 sc1\n\t"
      "s_branch Lstream_%=\n\t"
      "Lsetupc_%=:\n\t"
      "v_lshlrev_b32 v170, 4, v1\n\t"
      "s_cmp_lt_u32 s21, 4\n\t"
      "s_cselect_b32 s44, 0, 0\n\t"
      "v_xor_b32 v170, s44, v170\n\t"
      "v_add_u32 v171, 4096, v170\n\t"
      "v_add_u32 v168, 8192, v170\n\t"
      "v_lshlrev_b32 v169, 4, v1\n\t"
      "v_add_u32 v172, s88, v14\n\t"
      "v_add_u32 v173, s88, v15\n\t"
      "v_add_u32 v174, s89, v14\n\t"
      "v_add_u32 v175, s89, v15\n\t"
      "v_add_u32 v176, s90, v14\n\t"
      "v_add_u32 v177, s90, v15\n\t"
      "v_add_u32 v178, s91, v14\n\t"
      "v_add_u32 v179, s91, v15\n\t"
      "v_mov_b64 v[196:197], 0\n\t"
      "v_mov_b64 v[198:199], 0\n\t"
      "v_mov_b64 v[200:201], 0\n\t"
      "v_mov_b64 v[202:203], 0\n\t"
      "v_mov_b64 v[204:205], 0\n\t"
      "v_mov_b64 v[206:207], 0\n\t"
      "v_mov_b64 v[208:209], 0\n\t"
      "v_mov_b64 v[210:211], 0\n\t"
      "v_mov_b64 v[212:213], 0\n\t"
      "v_mov_b64 v[214:215], 0\n\t"
      "v_mov_b64 v[216:217], 0\n\t"
      "v_mov_b64 v[218:219], 0\n\t"
      "v_mov_b64 v[220:221], 0\n\t"
      "v_mov_b64 v[222:223], 0\n\t"
      "v_mov_b64 v[224:225], 0\n\t"
      "v_mov_b64 v[226:227], 0\n\t"
      "v_mov_b32 v193, 0\n\t"
      "s_setpc_b64 s[98:99]\n\t"
      "Lmval_%=:\n\t"
      "s_mov_b32 s47, 0\n\t"
      "Lmvl_%=:\n\t"
      "v_cmp_eq_u32 s[44:45], v52, v254\n\t"
      "s_and_b32 s44, s44, s35\n\t"
      "s_cmp_eq_u32 s44, s35\n\t"
      "s_cbranch_scc1 Lmvok_%=\n\t"
      "s_add_u32 s47, s47, 1\n\t"
      "s_cmp_gt_u32 s47, 200000\n\t"
      "s_cbranch_scc1 Lmvok_%=\n\t"
      "s_sleep 4\n\t"
      "s_mov_b32 exec_lo, s35\n\t"
      "s_mov_b32 exec_hi, 0\n\t"
      "global_load_dwordx2 v[52:53], v253, s[18:19] sc1\n\t"
      "global_load_dword v254, v255, s[18:19] sc1\n\t"
      "s_mov_b64 exec, -1\n\t"
      "s_waitcnt vmcnt(0)\n\t"
      "s_branch Lmvl_%=\n\t"
      "Lmvok_%=:\n\t"
      "v_readlane_b32 s44, v53, s34\n\t"
      "s_cmp_lt_u32 s43, 2\n\t"
      "s_cselect_b32 s45, 8, 7\n\t"
      "s_lshr_b32 s44, s44, 8\n\t"
      "s_add_u32 s44, s44, s45\n\t"
      "s_and_b32 s63, s44, 0xffffff\n\t"
      "s_setpc_b64 s[98:99]\n\t"
      "Lsetup_%=:\n\t"
      "s_lshr_b32 s44, s14, 7\n\t"
      "s_sub_u32 s44, 3, s44\n\t"
      "s_andn2_b32 s44, 7, s44\n\t"
      "s_xor_b32 s38, s38, s44\n\t"
      "s_lshl1_add_u32 s40, s36, s39\n\t"
      "s_mov_b32 s28, s14\n\t"
      "s_and_b32 s29, s15, 0xffff\n\t"
      "s_mov_b32 s30, 1409024\n\t"
      "s_mov_b32 s31, 0x20000\n\t"
      "s_mov_b32 s4, s18\n\t"
      "s_and_b32 s5, s19, 0xffff\n\t"
      "s_mov_b32 s6, 5636096\n\t"
      "s_mov_b32 s7, 0x20000\n\t"
      "v_lshrrev_b32 v3, 5, v1\n\t"
      "v_bfe_u32 v4, v1, 2, 2\n\t"
      "v_lshl_add_u32 v3, v3, 3, v4\n\t"
      "v_and_b32 v4, 3, v1\n\t"
      "v_lshlrev_b32 v4, 3, v4\n\t"
      "v_lshl_add_u32 v3, v3, 7, v4\n\t"
      "v_bfe_u32 v4, v1, 4, 1\n\t"
      "v_lshlrev_b32 v4, 5, v4\n\t"
      "v_lshlrev_b32 v5, 3, v1\n\t"
      "v_and_b32 v5, 64, v5\n\t"
      "v_xor_b32 v4, v4, v5\n\t"
      "v_add_u32 v14, v3, v4\n\t"
      "v_xor_b32 v15, 64, v14\n\t"
      "v_and_b32 v3, 15, v1\n\t"
      "s_lshl_b32 s44, s36, 12\n\t"
      "s_add_u32 s44, s44, 13697024\n\t"
      "v_lshl_add_u32 v252, v3, 8, s44\n\t"
      "s_lshl4_add_u32 s44, s36, s37\n\t"
      "s_lshl_b32 s45, s44, 12\n\t"
      "s_add_u32 s45, s45, 12582912\n\t"
      "v_lshl_add_u32 v253, v3, 8, s45\n\t"
      "s_lshl_b32 s44, s44, 8\n\t"
      "s_add_u32 s44, s44, 13631488\n\t"
      "v_mov_b32 v255, s44\n\t"
      "s_xor_b32 s34, s21, 4\n\t"
      "s_lshl3_add_u32 s34, s39, s34\n\t"
      "s_lshl_b32 s35, 1, s34\n\t"
      "s_lshl_b32 s44, s43, 12\n\t"
      "s_add_u32 s44, s44, 122880\n\t"
      "v_add_u32 v9, s44, v14\n\t"
      "v_add_u32 v10, s44, v15\n\t"
      "s_setpc_b64 s[98:99]\n\t"
      "Lsetupb_%=:\n\t"
      "v_lshrrev_b32 v3, 2, v1\n\t"
      "v_and_b32 v3, 4, v3\n\t"
      "v_and_b32 v4, 7, v1\n\t"
      "v_xor_b32 v3, v3, v4\n\t"
      "v_lshlrev_b32 v3, 4, v3\n\t"
      "v_lshrrev_b32 v4, 3, v1\n\t"
      "v_lshl_add_u32 v2, v4, 10, v3\n\t"
      "v_add_u32 v11, 7168, v2\n\t"
      "v_add_u32 v12, 14336, v2\n\t"
      "v_add_u32 v13, 21504, v2\n\t"
      "s_mul_i32 s62, s43, 344064\n\t"
      "s_lshl_b32 s44, s38, 7\n\t"
      "s_add_u32 s62, s62, s44\n\t"
      "s_cmp_lt_u32 s21, 4\n\t"
      "s_cselect_b32 s45, 1, 0\n\t"
      "s_cselect_b32 s46, 0, 0\n\t"
      "s_lshl1_add_u32 s45, s40, s45\n\t"
      "s_mul_i32 s45, s45, 88064\n\t"
      "s_mul_i32 s47, s43, 21504\n\t"
      "s_add_u32 s47, s47, s45\n\t"
      "s_add_u32 s60, s47, 20480\n\t"
      "s_lshl_b32 s48, s43, 10\n\t"
      "s_add_u32 s48, s48, s45\n\t"
      "s_add_u32 s48, s48, 86016\n\t"
      "s_cmp_lt_u32 s43, 2\n\t"
      "s_cselect_b32 s61, s48, 0x7ff00000\n\t"
      "s_mul_i32 s49, s43, 10240\n\t"
      "s_add_u32 s44, s46, 0\n\t"
      "s_and_b32 s44, s44, 3\n\t"
      "s_add_u32 s45, s44, s42\n\t"
      "s_and_b32 s45, s45, 3\n\t"
      "s_mul_i32 s45, s45, 5120\n\t"
      "s_add_u32 s56, s45, s47\n\t"
      "s_lshl_b32 s44, s44, 1\n\t"
      "s_lshr_b32 s44, 75, s44\n\t"
      "s_and_b32 s44, s44, 3\n\t"
      "s_mul_i32 s44, s44, 40960\n\t"
      "s_add_u32 s88, s44, s49\n\t"
      "s_add_u32 s44, s46, 1\n\t"
      "s_and_b32 s44, s44, 3\n\t"
      "s_add_u32 s45, s44, s42\n\t"
      "s_and_b32 s45, s45, 3\n\t"
      "s_mul_i32 s45, s45, 5120\n\t"
      "s_add_u32 s57, s45, s47\n\t"
      "s_lshl_b32 s44, s44, 1\n\t"
      "s_lshr_b32 s44, 75, s44\n\t"
      "s_and_b32 s44, s44, 3\n\t"
      "s_mul_i32 s44, s44, 40960\n\t"
      "s_add_u32 s89, s44, s49\n\t"
      "s_add_u32 s44, s46, 2\n\t"
      "s_and_b32 s44, s44, 3\n\t"
      "s_add_u32 s45, s44, s42\n\t"
      "s_and_b32 s45, s45, 3\n\t"
      "s_mul_i32 s45, s45, 5120\n\t"
      "s_add_u32 s58, s45, s47\n\t"
      "s_lshl_b32 s44, s44, 1\n\t"
      "s_lshr_b32 s44, 75, s44\n\t"
      "s_and_b32 s44, s44, 3\n\t"
      "s_mul_i32 s44, s44, 40960\n\t"
      "s_add_u32 s90, s44, s49\n\t"
      "s_add_u32 s44, s46, 3\n\t"
      "s_and_b32 s44, s44, 3\n\t"
      "s_add_u32 s45, s44, s42\n\t"
      "s_and_b32 s45, s45, 3\n\t"
      "s_mul_i32 s45, s45, 5120\n\t"
      "s_add_u32 s59, s45, s47\n\t"
      "s_lshl_b32 s44, s44, 1\n\t"
      "s_lshr_b32 s44, 75, s44\n\t"
      "s_and_b32 s44, s44, 3\n\t"
      "s_mul_i32 s44, s44, 40960\n\t"
      "s_add_u32 s91, s44, s49\n\t"
      "s_setpc_b64 s[98:99]\n\t"
      "Lhelp_%=:\n\t"
      "s_waitcnt lgkmcnt(0)\n\t"
      "s_call_b64 s[98:99], Lsetup_%=\n\t"
      "s_call_b64 s[98:99], Lsetupb_%=\n\t"
      "v_and_b32 v3, 15, v1\n\t"
      "s_lshl_b32 s46, s36, 12\n\t"
      "s_add_u32 s46, s46, 13631488\n\t"
      "v_lshl_add_u32 v239, v3, 8, s46\n\t"
      "s_lshl_b32 s46, s36, 16\n\t"
      "s_lshl_b32 s47, s37, 8\n\t"
      "s_add_u32 s46, s46, s47\n\t"
      "s_add_u32 s46, s46, 12582912\n\t"
      "v_lshl_add_u32 v240, v3, 12, s46\n\t"
      "v_mov_b32 v238, 0\n\t"
      "s_mov_b32 s35, 0xffff\n\t"
      "global_load_dwordx2 v[234:235], v253, s[18:19] sc1\n\t"
      "global_load_dword v236, v255, s[18:19] sc1\n\t"
      "s_lshl_b64 exec, 1, s37\n\t"
      "global_atomic_add v237, v252, v238, s[18:19] sc0 sc1\n\t"
      "s_mov_b64 exec, -1\n\t"
      "s_barrier\n\t"
      "s_barrier\n\t"
      "s_waitcnt vmcnt(0)\n\t"
      "v_lshlrev_b32 v234, v234, 1\n\t"
      "v_lshlrev_b32 v236, v236, 1\n\t"
      "v_or_b32 v234, v234, v236\n\t"
      "s_nop 1\n\t"
      "v_or_b32_dpp v234, v234, v234 row_shr:1 row_mask:0xf bank_mask:0xf bound_ctrl:0\n\t"
      "s_nop 1\n\t"
      "v_or_b32_dpp v234, v234, v234 row_shr:2 row_mask:0xf bank_mask:0xf bound_ctrl:0\n\t"
      "s_nop 1\n\t"
      "v_or_b32_dpp v234, v234, v234 row_shr:4 row_mask:0xf bank_mask:0xf bound_ctrl:0\n\t"
      "s_nop 1\n\t"
      "v_or_b32_dpp v234, v234, v234 row_shr:8 row_mask:0xf bank_mask:0xf bound_ctrl:0\n\t"
      "s_nop 0\n\t"
      "v_readlane_b32 s44, v234, 15\n\t"
      "v_readlane_b32 s45, v237, s37\n\t"
      "s_nop 3\n\t"
      "s_ff0_i32_b32 s96, s44\n\t"
      "s_lshl_b32 s45, s45, 8\n\t"
      "s_or_b32 s97, s96, s45\n\t"
      "v_mov_b32 v6, s96\n\t"
      "v_mov_b32 v243, s97\n\t"
      "s_mov_b64 exec, 1\n\t"
      "global_store_dword v255, v6, s[18:19] sc1\n\t"
      "s_mov_b64 exec, -1\n\t"
      "s_mul_i32 s44, s20, 6144\n\t"
      "v_lshlrev_b32 v3, 4, v1\n\t"
      "s_add_u32 s45, s44, 4096\n\t"
      "buffer_load_dwordx4 v[244:247], v3, s[28:31], s44 offen offset:0\n\t"
      "buffer_load_dwordx4 v[244:247], v3, s[28:31], s44 offen offset:1024\n\t"
      "buffer_load_dwordx4 v[244:247], v3, s[28:31], s44 offen offset:2048\n\t"
      "buffer_load_dwordx4 v[244:247], v3, s[28:31], s44 offen offset:3072\n\t"
      "buffer_load_dwordx4 v[244:247], v3, s[28:31], s45 offen offset:0\n\t"
      "buffer_load_dwordx4 v[244:247], v3, s[28:31], s45 offen offset:1024\n\t"
      "s_lshl_b32 s46, s38, 7\n\t"
      "s_barrier\n\t"
      "s_mov_b32 m0, 122880\n\t"
      "s_add_u32 s44, s46, 327680\n\t"
      "buffer_load_dwordx4 v2, s[28:31], s44 offen lds\n\t"
      "buffer_load_dwordx4 v11, s[28:31], s44 offen offset:1024 lds\n\t"
      "s_mov_b32 m0, 126976\n\t"
      "s_add_u32 s44, s46, 671744\n\t"
      "buffer_load_dwordx4 v2, s[28:31], s44 offen lds\n\t"
      "buffer_load_dwordx4 v11, s[28:31], s44 offen offset:1024 lds\n\t"
      "s_mov_b32 m0, 131072\n\t"
      "s_add_u32 s44, s46, 1015808\n\t"
      "buffer_load_dwordx4 v2, s[28:31], s44 offen lds\n\t"
      "buffer_load_dwordx4 v11, s[28:31], s44 offen offset:1024 lds\n\t"
      "s_barrier\n\t"
      "s_waitcnt vmcnt(12)\n\t"
      "global_load_dword v242, v239, s[18:19] sc1\n\t"
      "s_mov_b32 m0, 135168\n\t"
      "s_add_u32 s44, s46, 1359872\n\t"
      "buffer_load_dwordx4 v2, s[28:31], s44 offen lds\n\t"
      "buffer_load_dwordx4 v11, s[28:31], s44 offen offset:1024 lds\n\t"
      "s_mov_b32 m0, 124928\n\t"
      "s_add_u32 s44, s46, 1376256\n\t"
      "buffer_load_dwordx4 v2, s[28:31], s44 offen lds\n\t"
      "buffer_load_dwordx4 v11, s[28:31], s44 offen offset:1024 lds\n\t"
      "s_mov_b32 m0, 129024\n\t"
      "s_add_u32 s44, s46, 1392640\n\t"
      "buffer_load_dwordx4 v2, s[28:31], s44 offen lds\n\t"
      "buffer_load_dwordx4 v11, s[28:31], s44 offen offset:1024 lds\n\t"
      "s_barrier\n\t"
      "s_waitcnt vmcnt(0)\n\t"
      "s_mov_b64 exec, 0xffff\n\t"
      "global_store_dwordx2 v240, v[242:243], s[18:19] sc1\n\t"
      "s_mov_b64 exec, -1\n\t"
      "s_barrier\n\t"
      "ds_read_b64_tr_b16 v[32:33], v9 offset:0\n\t"
      "ds_read_b64_tr_b16 v[34:35], v9 offset:512\n\t"
      "ds_read_b64_tr_b16 v[36:37], v10 offset:0\n\t"
      "ds_read_b64_tr_b16 v[38:39], v10 offset:512\n\t"
      "ds_read_b64_tr_b16 v[40:41], v9 offset:2048\n\t"
      "ds_read_b64_tr_b16 v[42:43], v9 offset:2560\n\t"
      "ds_read_b64_tr_b16 v[44:45], v10 offset:2048\n\t"
      "ds_read_b64_tr_b16 v[46:47], v10 offset:2560\n\t"
      "s_waitcnt lgkmcnt(0)\n\t"
      "s_barrier\n\t"
      "s_branch Lwdtail_%=\n\t"
      "Lhchk_%=:\n\t"
      "s_mov_b32 s50, 0\n\t"
      "s_branch Lhfirst_%=\n\t"
      "Lhloop_%=:\n\t"
      "global_load_dwordx2 v[52:53], v253, s[18:19] sc1\n\t"
      "s_waitcnt vmcnt(0)\n\t"
      "Lhfirst_%=:\n\t"
      "v_and_b32 v235, 31, v53\n\t"
      "v_cmp_eq_u32 vcc, s96, v52\n\t"
      "v_cmp_ne_u32 s[44:45], v235, v242\n\t"
      "s_and_b64 vcc, vcc, 0xffff\n\t"
      "s_and_b64 s[44:45], s[44:45], vcc\n\t"
      "s_cbranch_scc0 Lhnofix_%=\n\t"
      "s_mov_b64 exec, s[44:45]\n\t"
      "v_mov_b32 v242, v235\n\t"
      "global_store_dwordx2 v240, v[242:243], s[18:19] sc1\n\t"
      "s_mov_b64 exec, -1\n\t"
      "Lhnofix_%=:\n\t"
      "s_cmp_eq_u32 vcc_lo, 0xffff\n\t"
      "s_cbranch_scc1 Labar8_%=\n\t"
      "s_add_u32 s50, s50, 1\n\t"
      "s_cmp_gt_u32 s50, 200000\n\t"
      "s_cbranch_scc1 Labar8_%=\n\t"
      "s_sleep 4\n\t"
      "s_branch Lhloop_%=\n\t"
      "Lcons_%=:\n\t"
      "s_waitcnt lgkmcnt(0)\n\t"
      "s_call_b64 s[98:99], Lsetup_%=\n\t"
      "s_lshr_b32 s48, s21, 1\n\t"
      "s_and_b32 s49, s21, 1\n\t"
      "s_mul_i32 s92, s37, 6\n\t"
      "s_mul_i32 s51, s49, 3\n\t"
      "s_add_u32 s92, s92, s51\n\t"
      "s_cmp_ge_u32 s92, 86\n\t"
      "s_cbranch_scc1 Lidle_%=\n\t"
      "s_lshl1_add_u32 s93, s37, s49\n\t"
      "s_mul_i32 s52, s93, 37\n\t"
      "s_lshr_b32 s52, s52, 8\n\t"
      "s_lshl_b32 s95, 1, s52\n\t"
      "s_cmp_eq_u32 s93, 28\n\t"
      "s_cselect_b32 s95, 3, s95\n\t"
      "s_lshl_b32 s52, s48, 3\n\t"
      "s_lshl_b32 s95, s95, s52\n\t"
      "s_or_b32 s35, s35, s95\n\t"
      "s_lshl_b32 s52, s95, 4\n\t"
      "s_or_b32 s35, s35, s52\n\t"
      "s_lshl1_add_u32 s50, s36, s48\n\t"
      "v_mov_b32 v50, 1\n\t"
      "s_cmp_lt_u32 s92, 84\n\t"
      "s_cselect_b32 s94, 0, 0x7ff00000\n\t"
      "s_mul_i32 s50, s50, 172\n\t"
      "s_add_u32 s92, s92, s50\n\t"
      "s_lshl_b32 s92, s92, 10\n\t"
      "s_add_u32 s93, s92, 1024\n\t"
      "s_add_u32 s94, s94, s92\n\t"
      "s_add_u32 s94, s94, 2048\n\t"
      "v_lshlrev_b32 v48, 4, v1\n\t"
      "v_mov_b32 v49, v48\n\t"
      "v_add_u32 v49, 88064, v49\n\t"
      "v_and_b32 v7, 15, v1\n\t"
      "v_bfe_u32 v8, v1, 1, 3\n\t"
      "v_lshrrev_b32 v56, 4, v1\n\t"
      "v_xor_b32 v8, v8, v56\n\t"
      "v_lshlrev_b32 v8, 4, v8\n\t"
      "v_lshl_add_u32 v8, v7, 7, v8\n\t"
      "s_lshl_b32 s48, s48, 13\n\t"
      "v_bfe_u32 v57, v1, 2, 2\n\t"
      "v_lshl_add_u32 v57, v56, 3, v57\n\t"
      "v_mul_u32_u24 v57, 192, v57\n\t"
      "v_and_b32 v58, 3, v1\n\t"
      "v_lshl_add_u32 v57, v58, 3, v57\n\t"
      "v_bfe_u32 v58, v1, 4, 1\n\t"
      "v_add_u32 v57, 16384, v57\n\t"
      "v_xor_b32 v59, s51, v58\n\t"
      "v_lshl_add_u32 v60, v59, 5, v57\n\t"
      "s_add_u32 s51, s51, 1\n\t"
      "v_xor_b32 v59, s51, v58\n\t"
      "v_lshl_add_u32 v61, v59, 5, v57\n\t"
      "v_add_u32 v62, 64, v60\n\t"
      "v_add_u32 v56, s48, v8\n\t"
      "v_xor_b32 v57, 64, v56\n\t"
      "s_mov_b64 vcc, 0\n\t"
      "v_mov_b64 v[64:65], 0\n\t"
      "v_mov_b64 v[66:67], 0\n\t"
      "v_mov_b64 v[68:69], 0\n\t"
      "v_mov_b64 v[70:71], 0\n\t"
      "v_mov_b64 v[72:73], 0\n\t"
      "v_mov_b64 v[74:75], 0\n\t"
      "v_mov_b64 v[76:77], 0\n\t"
      "v_mov_b64 v[78:79], 0\n\t"
      "v_mov_b64 v[80:81], 0\n\t"
      "v_mov_b64 v[82:83], 0\n\t"
      "v_mov_b64 v[84:85], 0\n\t"
      "v_mov_b64 v[86:87], 0\n\t"
      "v_mov_b64 v[88:89], 0\n\t"
      "v_mov_b64 v[90:91], 0\n\t"
      "v_mov_b64 v[92:93], 0\n\t"
      "v_mov_b64 v[94:95], 0\n\t"
      "v_mov_b64 v[96:97], 0\n\t"
      "v_mov_b64 v[98:99], 0\n\t"
      "v_mov_b64 v[100:101], 0\n\t"
      "v_mov_b64 v[102:103], 0\n\t"
      "v_mov_b64 v[104:105], 0\n\t"
      "v_mov_b64 v[106:107], 0\n\t"
      "v_mov_b64 v[108:109], 0\n\t"
      "v_mov_b64 v[110:111], 0\n\t"
      "v_mov_b64 v[112:113], 0\n\t"
      "v_mov_b64 v[114:115], 0\n\t"
      "v_mov_b64 v[116:117], 0\n\t"
      "v_mov_b64 v[118:119], 0\n\t"
      "v_mov_b64 v[120:121], 0\n\t"
      "v_mov_b64 v[122:123], 0\n\t"
      "v_mov_b64 v[124:125], 0\n\t"
      "v_mov_b64 v[126:127], 0\n\t"
      "v_mov_b64 v[128:129], 0\n\t"
      "v_mov_b64 v[130:131], 0\n\t"
      "v_mov_b64 v[132:133], 0\n\t"
      "v_mov_b64 v[134:135], 0\n\t"
      "v_mov_b64 v[136:137], 0\n\t"
      "v_mov_b64 v[138:139], 0\n\t"
      "v_mov_b64 v[140:141], 0\n\t"
      "v_mov_b64 v[142:143], 0\n\t"
      "v_mov_b64 v[144:145], 0\n\t"
      "v_mov_b64 v[146:147], 0\n\t"
      "v_mov_b64 v[148:149], 0\n\t"
      "v_mov_b64 v[150:151], 0\n\t"
      "v_mov_b64 v[152:153], 0\n\t"
      "v_mov_b64 v[154:155], 0\n\t"
      "v_mov_b64 v[156:157], 0\n\t"
      "v_mov_b64 v[158:159], 0\n\t"
      "v_mov_b32 v248, 0xbdd2d3e7\n\t"
      "v_mov_b32 v249, 0xbdd2d3e7\n\t"
      "v_mov_b32 v250, 0xc0135761\n\t"
      "v_mov_b32 v251, 0xc0135761\n\t"
      "s_mov_b32 s50, 0\n\t"
      "s_mov_b32 s52, 0\n\t"
      "s_waitcnt lgkmcnt(0)\n\t"
      "s_barrier\n\t"
      "ds_read_b64_tr_b16 v[176:177], v60 offset:0\n\t"
      "ds_read_b64_tr_b16 v[178:179], v60 offset:768\n\t"
      "ds_read_b128 v[160:163], v56 offset:0\n\t"
      "ds_read_b128 v[164:167], v56 offset:2048\n\t"
      "ds_read_b64_tr_b16 v[188:189], v60 offset:12288\n\t"
      "ds_read_b64_tr_b16 v[190:191], v60 offset:13056\n\t"
      "ds_read_b64_tr_b16 v[180:181], v61 offset:0\n\t"
      "ds_read_b64_tr_b16 v[182:183], v61 offset:768\n\t"
      "ds_read_b64_tr_b16 v[192:193], v61 offset:12288\n\t"
      "ds_read_b64_tr_b16 v[194:195], v61 offset:13056\n\t"
      "ds_read_b64_tr_b16 v[184:185], v62 offset:0\n\t"
      "ds_read_b64_tr_b16 v[186:187], v62 offset:768\n\t"
      "ds_read_b64_tr_b16 v[196:197], v62 offset:12288\n\t"
      "ds_read_b64_tr_b16 v[198:199], v62 offset:13056\n\t"
      "ds_read_b128 v[168:171], v56 offset:4096\n\t"
      "ds_read_b128 v[172:175], v56 offset:6144\n\t"
      "Lp1loop_%=:\n\t"
      "s_waitcnt lgkmcnt(0)\n\t"
      "v_mfma_f32_16x16x32_bf16 v[64:67], v[176:179], v[160:163], v[64:67]\n\t"
      "ds_read_b64_tr_b16 v[216:217], v60 offset:6144\n\t"
      "v_mfma_f32_16x16x32_bf16 v[68:71], v[176:179], v[164:167], v[68:71]\n\t"
      "ds_read_b64_tr_b16 v[218:219], v60 offset:6912\n\t"
      "v_mfma_f32_16x16x32_bf16 v[72:75], v[188:191], v[160:163], v[72:75]\n\t"
      "ds_read_b128 v[200:203], v57 offset:0\n\t"
      "v_mfma_f32_16x16x32_bf16 v[76:79], v[188:191], v[164:167], v[76:79]\n\t"
      "ds_read_b128 v[204:207], v57 offset:2048\n\t"
      "v_mfma_f32_16x16x32_bf16 v[80:83], v[180:183], v[160:163], v[80:83]\n\t"
      "ds_read_b64_tr_b16 v[228:229], v60 offset:18432\n\t"
      "v_mfma_f32_16x16x32_bf16 v[84:87], v[180:183], v[164:167], v[84:87]\n\t"
      "ds_read_b64_tr_b16 v[230:231], v60 offset:19200\n\t"
      "v_mfma_f32_16x16x32_bf16 v[88:91], v[192:195], v[160:163], v[88:91]\n\t"
      "ds_read_b64_tr_b16 v[220:221], v61 offset:6144\n\t"
      "v_mfma_f32_16x16x32_bf16 v[92:95], v[192:195], v[164:167], v[92:95]\n\t"
      "ds_read_b64_tr_b16 v[222:223], v61 offset:6912\n\t"
      "v_mfma_f32_16x16x32_bf16 v[96:99], v[184:187], v[160:163], v[96:99]\n\t"
      "ds_read_b64_tr_b16 v[232:233], v61 offset:18432\n\t"
      "v_mfma_f32_16x16x32_bf16 v[100:103], v[184:187], v[164:167], v[100:103]\n\t"
      "ds_read_b64_tr_b16 v[234:235], v61 offset:19200\n\t"
      "v_mfma_f32_16x16x32_bf16 v[104:107], v[196:199], v[160:163], v[104:107]\n\t"
      "ds_read_b64_tr_b16 v[224:225], v62 offset:6144\n\t"
      "v_mfma_f32_16x16x32_bf16 v[108:111], v[196:199], v[164:167], v[108:111]\n\t"
      "ds_read_b64_tr_b16 v[226:227], v62 offset:6912\n\t"
      "v_mfma_f32_16x16x32_bf16 v[112:115], v[176:179], v[168:171], v[112:115]\n\t"
      "ds_read_b64_tr_b16 v[236:237], v62 offset:18432\n\t"
      "v_mfma_f32_16x16x32_bf16 v[116:119], v[176:179], v[172:175], v[116:119]\n\t"
      "ds_read_b64_tr_b16 v[238:239], v62 offset:19200\n\t"
      "v_mfma_f32_16x16x32_bf16 v[120:123], v[188:191], v[168:171], v[120:123]\n\t"
      "ds_read_b128 v[208:211], v57 offset:4096\n\t"
      "v_mfma_f32_16x16x32_bf16 v[124:127], v[188:191], v[172:175], v[124:127]\n\t"
      "ds_read_b128 v[212:215], v57 offset:6144\n\t"
      "v_mfma_f32_16x16x32_bf16 v[128:131], v[180:183], v[168:171], v[128:131]\n\t"
      "v_mfma_f32_16x16x32_bf16 v[132:135], v[180:183], v[172:175], v[132:135]\n\t"
      "s_add_u32 s52, s52, 1\n\t"
      "s_cmp_eq_u32 s52, 3\n\t"
      "v_mfma_f32_16x16x32_bf16 v[136:139], v[192:195], v[168:171], v[136:139]\n\t"
      "s_cselect_b32 s51, -81920, 0\n\t"
      "s_cselect_b32 s52, 0, s52\n\t"
      "v_mfma_f32_16x16x32_bf16 v[140:143], v[192:195], v[172:175], v[140:143]\n\t"
      "s_cselect_b32 s51, s51, 40960\n\t"
      "s_add_u32 s50, s50, 1\n\t"
      "v_mfma_f32_16x16x32_bf16 v[144:147], v[184:187], v[168:171], v[144:147]\n\t"
      "v_mfma_f32_16x16x32_bf16 v[148:151], v[184:187], v[172:175], v[148:151]\n\t"
      "v_mfma_f32_16x16x32_bf16 v[152:155], v[196:199], v[168:171], v[152:155]\n\t"
      "v_mfma_f32_16x16x32_bf16 v[156:159], v[196:199], v[172:175], v[156:159]\n\t"
      "s_waitcnt lgkmcnt(0)\n\t"
      "v_mfma_f32_16x16x32_bf16 v[64:67], v[216:219], v[200:203], v[64:67]\n\t"
      "v_add_u32 v56, s51, v56\n\t"
      "v_mfma_f32_16x16x32_bf16 v[68:71], v[216:219], v[204:207], v[68:71]\n\t"
      "v_add_u32 v60, s51, v60\n\t"
      "v_mfma_f32_16x16x32_bf16 v[72:75], v[228:231], v[200:203], v[72:75]\n\t"
      "v_add_u32 v61, s51, v61\n\t"
      "v_mfma_f32_16x16x32_bf16 v[76:79], v[228:231], v[204:207], v[76:79]\n\t"
      "v_add_u32 v62, s51, v62\n\t"
      "v_mfma_f32_16x16x32_bf16 v[80:83], v[220:223], v[200:203], v[80:83]\n\t"
      "v_add_u32 v57, s51, v57\n\t"
      "v_mfma_f32_16x16x32_bf16 v[84:87], v[220:223], v[204:207], v[84:87]\n\t"
      "v_mfma_f32_16x16x32_bf16 v[88:91], v[232:235], v[200:203], v[88:91]\n\t"
      "v_mfma_f32_16x16x32_bf16 v[92:95], v[232:235], v[204:207], v[92:95]\n\t"
      "v_mfma_f32_16x16x32_bf16 v[96:99], v[224:227], v[200:203], v[96:99]\n\t"
      "v_mfma_f32_16x16x32_bf16 v[100:103], v[224:227], v[204:207], v[100:103]\n\t"
      "v_mfma_f32_16x16x32_bf16 v[104:107], v[236:239], v[200:203], v[104:107]\n\t"
      "v_mfma_f32_16x16x32_bf16 v[108:111], v[236:239], v[204:207], v[108:111]\n\t"
      "s_barrier\n\t"
      "ds_read_b64_tr_b16 v[176:177], v60 offset:0\n\t"
      "ds_read_b64_tr_b16 v[178:179], v60 offset:768\n\t"
      "v_mfma_f32_16x16x32_bf16 v[112:115], v[216:219], v[208:211], v[112:115]\n\t"
      "ds_read_b128 v[160:163], v56 offset:0\n\t"
      "ds_read_b128 v[164:167], v56 offset:2048\n\t"
      "v_mfma_f32_16x16x32_bf16 v[116:119], v[216:219], v[212:215], v[116:119]\n\t"
      "ds_read_b64_tr_b16 v[188:189], v60 offset:12288\n\t"
      "ds_read_b64_tr_b16 v[190:191], v60 offset:13056\n\t"
      "v_mfma_f32_16x16x32_bf16 v[120:123], v[228:231], v[208:211], v[120:123]\n\t"
      "ds_read_b64_tr_b16 v[180:181], v61 offset:0\n\t"
      "ds_read_b64_tr_b16 v[182:183], v61 offset:768\n\t"
      "v_mfma_f32_16x16x32_bf16 v[124:127], v[228:231], v[212:215], v[124:127]\n\t"
      "ds_read_b64_tr_b16 v[192:193], v61 offset:12288\n\t"
      "ds_read_b64_tr_b16 v[194:195], v61 offset:13056\n\t"
      "v_mfma_f32_16x16x32_bf16 v[128:131], v[220:223], v[208:211], v[128:131]\n\t"
      "ds_read_b64_tr_b16 v[184:185], v62 offset:0\n\t"
      "ds_read_b64_tr_b16 v[186:187], v62 offset:768\n\t"
      "v_mfma_f32_16x16x32_bf16 v[132:135], v[220:223], v[212:215], v[132:135]\n\t"
      "ds_read_b64_tr_b16 v[196:197], v62 offset:12288\n\t"
      "ds_read_b64_tr_b16 v[198:199], v62 offset:13056\n\t"
      "v_mfma_f32_16x16x32_bf16 v[136:139], v[232:235], v[208:211], v[136:139]\n\t"
      "ds_read_b128 v[168:171], v56 offset:4096\n\t"
      "ds_read_b128 v[172:175], v56 offset:6144\n\t"
      "v_mfma_f32_16x16x32_bf16 v[140:143], v[232:235], v[212:215], v[140:143]\n\t"
      "v_mfma_f32_16x16x32_bf16 v[144:147], v[224:227], v[208:211], v[144:147]\n\t"
      "v_mfma_f32_16x16x32_bf16 v[148:151], v[224:227], v[212:215], v[148:151]\n\t"
      "v_mfma_f32_16x16x32_bf16 v[152:155], v[236:239], v[208:211], v[152:155]\n\t"
      "s_cmp_eq_u32 s50, 5\n\t"
      "s_cbranch_scc0 Lp1nc_%=\n\t"
      "s_waitcnt lgkmcnt(6)\n\t"
      "ds_read_b64_tr_b16 v[32:33], v9 offset:0\n\t"
      "ds_read_b64_tr_b16 v[34:35], v9 offset:512\n\t"
      "ds_read_b64_tr_b16 v[36:37], v10 offset:0\n\t"
      "ds_read_b64_tr_b16 v[38:39], v10 offset:512\n\t"
      "ds_read_b64_tr_b16 v[40:41], v9 offset:2048\n\t"
      "ds_read_b64_tr_b16 v[42:43], v9 offset:2560\n\t"
      "ds_read_b64_tr_b16 v[44:45], v10 offset:2048\n\t"
      "ds_read_b64_tr_b16 v[46:47], v10 offset:2560\n\t"
      "Lp1nc_%=:\n\t"
      "v_mfma_f32_16x16x32_bf16 v[156:159], v[236:239], v[212:215], v[156:159]\n\t"
      "s_cmp_lt_u32 s50, 7\n\t"
      "s_cbranch_scc1 Lp1loop_%=\n\t"
      "s_waitcnt lgkmcnt(8)\n\t"
      "ds_read_b64_tr_b16 v[216:217], v60 offset:6144\n\t"
      "ds_read_b64_tr_b16 v[218:219], v60 offset:6912\n\t"
      "ds_read_b128 v[200:203], v57 offset:0\n\t"
      "ds_read_b128 v[204:207], v57 offset:2048\n\t"
      "ds_read_b64_tr_b16 v[228:229], v60 offset:18432\n\t"
      "ds_read_b64_tr_b16 v[230:231], v60 offset:19200\n\t"
      "s_mov_b32 exec_lo, s35\n\t"
      "s_mov_b32 exec_hi, 0\n\t"
      "global_load_dwordx2 v[52:53], v253, s[18:19] sc1\n\t"
      "global_load_dword v254, v255, s[18:19] sc1\n\t"
      "s_mov_b64 exec, -1\n\t"
      "s_nop 3\n\t"
      "s_waitcnt lgkmcnt(6)\n\t"
      "v_mfma_f32_16x16x32_bf16 v[64:67], v[176:179], v[160:163], v[64:67]\n\t"
      "ds_read_b64_tr_b16 v[220:221], v61 offset:6144\n\t"
      "v_mfma_f32_16x16x32_bf16 v[68:71], v[176:179], v[164:167], v[68:71]\n\t"
      "ds_read_b64_tr_b16 v[222:223], v61 offset:6912\n\t"
      "v_mfma_f32_16x16x32_bf16 v[72:75], v[188:191], v[160:163], v[72:75]\n\t"
      "ds_read_b64_tr_b16 v[232:233], v61 offset:18432\n\t"
      "v_mfma_f32_16x16x32_bf16 v[76:79], v[188:191], v[164:167], v[76:79]\n\t"
      "ds_read_b64_tr_b16 v[234:235], v61 offset:19200\n\t"
      "v_mfma_f32_16x16x32_bf16 v[80:83], v[180:183], v[160:163], v[80:83]\n\t"
      "ds_read_b64_tr_b16 v[224:225], v62 offset:6144\n\t"
      "v_mfma_f32_16x16x32_bf16 v[84:87], v[180:183], v[164:167], v[84:87]\n\t"
      "ds_read_b64_tr_b16 v[226:227], v62 offset:6912\n\t"
      "v_mfma_f32_16x16x32_bf16 v[88:91], v[192:195], v[160:163], v[88:91]\n\t"
      "ds_read_b64_tr_b16 v[236:237], v62 offset:18432\n\t"
      "ds_read_b64_tr_b16 v[238:239], v62 offset:19200\n\t"
      "v_mfma_f32_16x16x32_bf16 v[92:95], v[192:195], v[164:167], v[92:95]\n\t"
      "ds_read_b128 v[208:211], v57 offset:4096\n\t"
      "ds_read_b128 v[212:215], v57 offset:6144\n\t"
      "s_waitcnt lgkmcnt(10)\n\t"
      "v_mfma_f32_16x16x32_bf16 v[64:67], v[216:219], v[200:203], v[64:67]\n\t"
      "v_mfma_f32_16x16x32_bf16 v[68:71], v[216:219], v[204:207], v[68:71]\n\t"
      "v_mfma_f32_16x16x32_bf16 v[72:75], v[228:231], v[200:203], v[72:75]\n\t"
      "v_mfma_f32_16x16x32_bf16 v[76:79], v[228:231], v[204:207], v[76:79]\n\t"
      "s_waitcnt lgkmcnt(6)\n\t"
      "v_mfma_f32_16x16x32_bf16 v[80:83], v[220:223], v[200:203], v[80:83]\n\t"
      "v_mfma_f32_16x16x32_bf16 v[84:87], v[220:223], v[204:207], v[84:87]\n\t"
      "v_mfma_f32_16x16x32_bf16 v[88:91], v[232:235], v[200:203], v[88:91]\n\t"
      "v_mfma_f32_16x16x32_bf16 v[92:95], v[232:235], v[204:207], v[92:95]\n\t"
      "s_nop 3\n\t"
      "v_pk_mul_f32 v[240:241], v[64:65], v[64:65]\n\t"
      "v_pk_mul_f32 v[242:243], v[66:67], v[66:67]\n\t"
      "v_pk_mul_f32 v[244:245], v[68:69], v[68:69]\n\t"
      "v_pk_mul_f32 v[246:247], v[70:71], v[70:71]\n\t"
      "v_pk_fma_f32 v[240:241], v[240:241], v[248:249], v[250:251]\n\t"
      "v_pk_fma_f32 v[242:243], v[242:243], v[248:249], v[250:251]\n\t"
      "v_pk_fma_f32 v[244:245], v[244:245], v[248:249], v[250:251]\n\t"
      "v_pk_fma_f32 v[246:247], v[246:247], v[248:249], v[250:251]\n\t"
      "v_pk_mul_f32 v[240:241], v[240:241], v[64:65]\n\t"
      "v_pk_mul_f32 v[242:243], v[242:243], v[66:67]\n\t"
      "v_pk_mul_f32 v[244:245], v[244:245], v[68:69]\n\t"
      "v_pk_mul_f32 v[246:247], v[246:247], v[70:71]\n\t"
      "v_pk_mul_f32 v[72:73], v[64:65], v[72:73]\n\t"
      "v_pk_mul_f32 v[74:75], v[66:67], v[74:75]\n\t"
      "v_pk_mul_f32 v[76:77], v[68:69], v[76:77]\n\t"
      "v_pk_mul_f32 v[78:79], v[70:71], v[78:79]\n\t"
      "s_waitcnt lgkmcnt(0)\n\t"
      "v_mfma_f32_16x16x32_bf16 v[96:99], v[184:187], v[160:163], v[96:99]\n\t"
      "v_exp_f32 v240, v240\n\t"
      "v_mfma_f32_16x16x32_bf16 v[100:103], v[184:187], v[164:167], v[100:103]\n\t"
      "v_exp_f32 v241, v241\n\t"
      "v_mfma_f32_16x16x32_bf16 v[104:107], v[196:199], v[160:163], v[104:107]\n\t"
      "v_exp_f32 v242, v242\n\t"
      "v_mfma_f32_16x16x32_bf16 v[108:111], v[196:199], v[164:167], v[108:111]\n\t"
      "v_exp_f32 v243, v243\n\t"
      "v_exp_f32 v244, v244\n\t"
      "v_exp_f32 v245, v245\n\t"
      "v_exp_f32 v246, v246\n\t"
      "v_exp_f32 v247, v247\n\t"
      "v_pk_add_f32 v[240:241], v[240:241], 1.0 op_sel_hi:[1,0]\n\t"
      "v_pk_add_f32 v[242:243], v[242:243], 1.0 op_sel_hi:[1,0]\n\t"
      "v_pk_add_f32 v[244:245], v[244:245], 1.0 op_sel_hi:[1,0]\n\t"
      "v_pk_add_f32 v[246:247], v[246:247], 1.0 op_sel_hi:[1,0]\n\t"
      "v_mfma_f32_16x16x32_bf16 v[96:99], v[224:227], v[200:203], v[96:99]\n\t"
      "v_rcp_f32 v240, v240\n\t"
      "v_mfma_f32_16x16x32_bf16 v[100:103], v[224:227], v[204:207], v[100:103]\n\t"
      "v_rcp_f32 v241, v241\n\t"
      "v_mfma_f32_16x16x32_bf16 v[104:107], v[236:239], v[200:203], v[104:107]\n\t"
      "v_rcp_f32 v242, v242\n\t"
      "v_mfma_f32_16x16x32_bf16 v[108:111], v[236:239], v[204:207], v[108:111]\n\t"
      "v_rcp_f32 v243, v243\n\t"
      "v_rcp_f32 v244, v244\n\t"
      "v_rcp_f32 v245, v245\n\t"
      "v_rcp_f32 v246, v246\n\t"
      "v_rcp_f32 v247, v247\n\t"
      "v_pk_mul_f32 v[72:73], v[72:73], v[240:241]\n\t"
      "v_pk_mul_f32 v[74:75], v[74:75], v[242:243]\n\t"
      "v_pk_mul_f32 v[76:77], v[76:77], v[244:245]\n\t"
      "v_pk_mul_f32 v[78:79], v[78:79], v[246:247]\n\t"
      "v_cvt_pk_bf16_f32 v64, v72, v73\n\t"
      "v_cvt_pk_bf16_f32 v66, v76, v77\n\t"
      "v_cvt_pk_bf16_f32 v65, v74, v75\n\t"
      "v_cvt_pk_bf16_f32 v67, v78, v79\n\t"
      "s_nop 0\n\t"
      "v_permlane16_swap_b32 v64, v66\n\t"
      "v_permlane16_swap_b32 v65, v67\n\t"
      "buffer_store_dwordx4 v[64:67], v48, s[4:7], s92 offen sc1\n\t"
      "v_pk_mul_f32 v[240:241], v[80:81], v[80:81]\n\t"
      "v_pk_mul_f32 v[242:243], v[82:83], v[82:83]\n\t"
      "v_pk_mul_f32 v[244:245], v[84:85], v[84:85]\n\t"
      "v_pk_mul_f32 v[246:247], v[86:87], v[86:87]\n\t"
      "v_pk_fma_f32 v[240:241], v[240:241], v[248:249], v[250:251]\n\t"
      "v_pk_fma_f32 v[242:243], v[242:243], v[248:249], v[250:251]\n\t"
      "v_pk_fma_f32 v[244:245], v[244:245], v[248:249], v[250:251]\n\t"
      "v_pk_fma_f32 v[246:247], v[246:247], v[248:249], v[250:251]\n\t"
      "v_pk_mul_f32 v[240:241], v[240:241], v[80:81]\n\t"
      "v_pk_mul_f32 v[242:243], v[242:243], v[82:83]\n\t"
      "v_pk_mul_f32 v[244:245], v[244:245], v[84:85]\n\t"
      "v_pk_mul_f32 v[246:247], v[246:247], v[86:87]\n\t"
      "v_pk_mul_f32 v[88:89], v[80:81], v[88:89]\n\t"
      "v_pk_mul_f32 v[90:91], v[82:83], v[90:91]\n\t"
      "v_pk_mul_f32 v[92:93], v[84:85], v[92:93]\n\t"
      "v_pk_mul_f32 v[94:95], v[86:87], v[94:95]\n\t"
      "v_mfma_f32_16x16x32_bf16 v[112:115], v[176:179], v[168:171], v[112:115]\n\t"
      "v_exp_f32 v240, v240\n\t"
      "v_mfma_f32_16x16x32_bf16 v[116:119], v[176:179], v[172:175], v[116:119]\n\t"
      "v_exp_f32 v241, v241\n\t"
      "v_mfma_f32_16x16x32_bf16 v[120:123], v[188:191], v[168:171], v[120:123]\n\t"
      "v_exp_f32 v242, v242\n\t"
      "v_mfma_f32_16x16x32_bf16 v[124:127], v[188:191], v[172:175], v[124:127]\n\t"
      "v_exp_f32 v243, v243\n\t"
      "v_exp_f32 v244, v244\n\t"
      "v_exp_f32 v245, v245\n\t"
      "v_exp_f32 v246, v246\n\t"
      "v_exp_f32 v247, v247\n\t"
      "v_pk_add_f32 v[240:241], v[240:241], 1.0 op_sel_hi:[1,0]\n\t"
      "v_pk_add_f32 v[242:243], v[242:243], 1.0 op_sel_hi:[1,0]\n\t"
      "v_pk_add_f32 v[244:245], v[244:245], 1.0 op_sel_hi:[1,0]\n\t"
      "v_pk_add_f32 v[246:247], v[246:247], 1.0 op_sel_hi:[1,0]\n\t"
      "v_mfma_f32_16x16x32_bf16 v[112:115], v[216:219], v[208:211], v[112:115]\n\t"
      "v_rcp_f32 v240, v240\n\t"
      "v_mfma_f32_16x16x32_bf16 v[116:119], v[216:219], v[212:215], v[116:119]\n\t"
      "v_rcp_f32 v241, v241\n\t"
      "v_mfma_f32_16x16x32_bf16 v[120:123], v[228:231], v[208:211], v[120:123]\n\t"
      "v_rcp_f32 v242, v242\n\t"
      "v_mfma_f32_16x16x32_bf16 v[124:127], v[228:231], v[212:215], v[124:127]\n\t"
      "v_rcp_f32 v243, v243\n\t"
      "v_rcp_f32 v244, v244\n\t"
      "v_rcp_f32 v245, v245\n\t"
      "v_rcp_f32 v246, v246\n\t"
      "v_rcp_f32 v247, v247\n\t"
      "v_pk_mul_f32 v[88:89], v[88:89], v[240:241]\n\t"
      "v_pk_mul_f32 v[90:91], v[90:91], v[242:243]\n\t"
      "v_pk_mul_f32 v[92:93], v[92:93], v[244:245]\n\t"
      "v_pk_mul_f32 v[94:95], v[94:95], v[246:247]\n\t"
      "v_cvt_pk_bf16_f32 v80, v88, v89\n\t"
      "v_cvt_pk_bf16_f32 v82, v92, v93\n\t"
      "v_cvt_pk_bf16_f32 v81, v90, v91\n\t"
      "v_cvt_pk_bf16_f32 v83, v94, v95\n\t"
      "s_nop 0\n\t"
      "v_permlane16_swap_b32 v80, v82\n\t"
      "v_permlane16_swap_b32 v81, v83\n\t"
      "buffer_store_dwordx4 v[80:83], v48, s[4:7], s93 offen sc1\n\t"
      "v_pk_mul_f32 v[240:241], v[96:97], v[96:97]\n\t"
      "v_pk_mul_f32 v[242:243], v[98:99], v[98:99]\n\t"
      "v_pk_mul_f32 v[244:245], v[100:101], v[100:101]\n\t"
      "v_pk_mul_f32 v[246:247], v[102:103], v[102:103]\n\t"
      "v_pk_fma_f32 v[240:241], v[240:241], v[248:249], v[250:251]\n\t"
      "v_pk_fma_f32 v[242:243], v[242:243], v[248:249], v[250:251]\n\t"
      "v_pk_fma_f32 v[244:245], v[244:245], v[248:249], v[250:251]\n\t"
      "v_pk_fma_f32 v[246:247], v[246:247], v[248:249], v[250:251]\n\t"
      "v_pk_mul_f32 v[240:241], v[240:241], v[96:97]\n\t"
      "v_pk_mul_f32 v[242:243], v[242:243], v[98:99]\n\t"
      "v_pk_mul_f32 v[244:245], v[244:245], v[100:101]\n\t"
      "v_pk_mul_f32 v[246:247], v[246:247], v[102:103]\n\t"
      "v_pk_mul_f32 v[104:105], v[96:97], v[104:105]\n\t"
      "v_pk_mul_f32 v[106:107], v[98:99], v[106:107]\n\t"
      "v_pk_mul_f32 v[108:109], v[100:101], v[108:109]\n\t"
      "v_pk_mul_f32 v[110:111], v[102:103], v[110:111]\n\t"
      "v_mfma_f32_16x16x32_bf16 v[128:131], v[180:183], v[168:171], v[128:131]\n\t"
      "v_exp_f32 v240, v240\n\t"
      "v_mfma_f32_16x16x32_bf16 v[132:135], v[180:183], v[172:175], v[132:135]\n\t"
      "v_exp_f32 v241, v241\n\t"
      "v_mfma_f32_16x16x32_bf16 v[136:139], v[192:195], v[168:171], v[136:139]\n\t"
      "v_exp_f32 v242, v242\n\t"
      "v_mfma_f32_16x16x32_bf16 v[140:143], v[192:195], v[172:175], v[140:143]\n\t"
      "v_exp_f32 v243, v243\n\t"
      "v_exp_f32 v244, v244\n\t"
      "v_exp_f32 v245, v245\n\t"
      "v_exp_f32 v246, v246\n\t"
      "v_exp_f32 v247, v247\n\t"
      "v_pk_add_f32 v[240:241], v[240:241], 1.0 op_sel_hi:[1,0]\n\t"
      "v_pk_add_f32 v[242:243], v[242:243], 1.0 op_sel_hi:[1,0]\n\t"
      "v_pk_add_f32 v[244:245], v[244:245], 1.0 op_sel_hi:[1,0]\n\t"
      "v_pk_add_f32 v[246:247], v[246:247], 1.0 op_sel_hi:[1,0]\n\t"
      "v_mfma_f32_16x16x32_bf16 v[128:131], v[220:223], v[208:211], v[128:131]\n\t"
      "v_rcp_f32 v240, v240\n\t"
      "v_mfma_f32_16x16x32_bf16 v[132:135], v[220:223], v[212:215], v[132:135]\n\t"
      "v_rcp_f32 v241, v241\n\t"
      "v_mfma_f32_16x16x32_bf16 v[136:139], v[232:235], v[208:211], v[136:139]\n\t"
      "v_rcp_f32 v242, v242\n\t"
      "v_mfma_f32_16x16x32_bf16 v[140:143], v[232:235], v[212:215], v[140:143]\n\t"
      "v_rcp_f32 v243, v243\n\t"
      "v_rcp_f32 v244, v244\n\t"
      "v_rcp_f32 v245, v245\n\t"
      "v_rcp_f32 v246, v246\n\t"
      "v_rcp_f32 v247, v247\n\t"
      "v_pk_mul_f32 v[104:105], v[104:105], v[240:241]\n\t"
      "v_pk_mul_f32 v[106:107], v[106:107], v[242:243]\n\t"
      "v_pk_mul_f32 v[108:109], v[108:109], v[244:245]\n\t"
      "v_pk_mul_f32 v[110:111], v[110:111], v[246:247]\n\t"
      "v_cvt_pk_bf16_f32 v96, v104, v105\n\t"
      "v_cvt_pk_bf16_f32 v98, v108, v109\n\t"
      "v_cvt_pk_bf16_f32 v97, v106, v107\n\t"
      "v_cvt_pk_bf16_f32 v99, v110, v111\n\t"
      "s_nop 0\n\t"
      "v_permlane16_swap_b32 v96, v98\n\t"
      "v_permlane16_swap_b32 v97, v99\n\t"
      "buffer_store_dwordx4 v[96:99], v48, s[4:7], s94 offen sc1\n\t"
      "v_pk_mul_f32 v[240:241], v[112:113], v[112:113]\n\t"
      "v_pk_mul_f32 v[242:243], v[114:115], v[114:115]\n\t"
      "v_pk_mul_f32 v[244:245], v[116:117], v[116:117]\n\t"
      "v_pk_mul_f32 v[246:247], v[118:119], v[118:119]\n\t"
      "v_pk_fma_f32 v[240:241], v[240:241], v[248:249], v[250:251]\n\t"
      "v_pk_fma_f32 v[242:243], v[242:243], v[248:249], v[250:251]\n\t"
      "v_pk_fma_f32 v[244:245], v[244:245], v[248:249], v[250:251]\n\t"
      "v_pk_fma_f32 v[246:247], v[246:247], v[248:249], v[250:251]\n\t"
      "v_pk_mul_f32 v[240:241], v[240:241], v[112:113]\n\t"
      "v_pk_mul_f32 v[242:243], v[242:243], v[114:115]\n\t"
      "v_pk_mul_f32 v[244:245], v[244:245], v[116:117]\n\t"
      "v_pk_mul_f32 v[246:247], v[246:247], v[118:119]\n\t"
      "v_pk_mul_f32 v[120:121], v[112:113], v[120:121]\n\t"
      "v_pk_mul_f32 v[122:123], v[114:115], v[122:123]\n\t"
      "v_pk_mul_f32 v[124:125], v[116:117], v[124:125]\n\t"
      "v_pk_mul_f32 v[126:127], v[118:119], v[126:127]\n\t"
      "v_mfma_f32_16x16x32_bf16 v[144:147], v[184:187], v[168:171], v[144:147]\n\t"
      "v_exp_f32 v240, v240\n\t"
      "v_mfma_f32_16x16x32_bf16 v[148:151], v[184:187], v[172:175], v[148:151]\n\t"
      "v_exp_f32 v241, v241\n\t"
      "v_mfma_f32_16x16x32_bf16 v[152:155], v[196:199], v[168:171], v[152:155]\n\t"
      "v_exp_f32 v242, v242\n\t"
      "v_mfma_f32_16x16x32_bf16 v[156:159], v[196:199], v[172:175], v[156:159]\n\t"
      "v_exp_f32 v243, v243\n\t"
      "v_exp_f32 v244, v244\n\t"
      "v_exp_f32 v245, v245\n\t"
      "v_exp_f32 v246, v246\n\t"
      "v_exp_f32 v247, v247\n\t"
      "v_pk_add_f32 v[240:241], v[240:241], 1.0 op_sel_hi:[1,0]\n\t"
      "v_pk_add_f32 v[242:243], v[242:243], 1.0 op_sel_hi:[1,0]\n\t"
      "v_pk_add_f32 v[244:245], v[244:245], 1.0 op_sel_hi:[1,0]\n\t"
      "v_pk_add_f32 v[246:247], v[246:247], 1.0 op_sel_hi:[1,0]\n\t"
      "v_mfma_f32_16x16x32_bf16 v[144:147], v[224:227], v[208:211], v[144:147]\n\t"
      "v_rcp_f32 v240, v240\n\t"
      "v_mfma_f32_16x16x32_bf16 v[148:151], v[224:227], v[212:215], v[148:151]\n\t"
      "v_rcp_f32 v241, v241\n\t"
      "v_mfma_f32_16x16x32_bf16 v[152:155], v[236:239], v[208:211], v[152:155]\n\t"
      "v_rcp_f32 v242, v242\n\t"
      "v_mfma_f32_16x16x32_bf16 v[156:159], v[236:239], v[212:215], v[156:159]\n\t"
      "v_rcp_f32 v243, v243\n\t"
      "v_rcp_f32 v244, v244\n\t"
      "v_rcp_f32 v245, v245\n\t"
      "v_rcp_f32 v246, v246\n\t"
      "v_rcp_f32 v247, v247\n\t"
      "v_pk_mul_f32 v[120:121], v[120:121], v[240:241]\n\t"
      "v_pk_mul_f32 v[122:123], v[122:123], v[242:243]\n\t"
      "v_pk_mul_f32 v[124:125], v[124:125], v[244:245]\n\t"
      "v_pk_mul_f32 v[126:127], v[126:127], v[246:247]\n\t"
      "v_cvt_pk_bf16_f32 v112, v120, v121\n\t"
      "v_cvt_pk_bf16_f32 v114, v124, v125\n\t"
      "v_cvt_pk_bf16_f32 v113, v122, v123\n\t"
      "v_cvt_pk_bf16_f32 v115, v126, v127\n\t"
      "s_nop 0\n\t"
      "v_permlane16_swap_b32 v112, v114\n\t"
      "v_permlane16_swap_b32 v113, v115\n\t"
      "buffer_store_dwordx4 v[112:115], v49, s[4:7], s92 offen sc1\n\t"
      "v_pk_mul_f32 v[240:241], v[128:129], v[128:129]\n\t"
      "v_pk_mul_f32 v[242:243], v[130:131], v[130:131]\n\t"
      "v_pk_mul_f32 v[244:245], v[132:133], v[132:133]\n\t"
      "v_pk_mul_f32 v[246:247], v[134:135], v[134:135]\n\t"
      "v_pk_fma_f32 v[240:241], v[240:241], v[248:249], v[250:251]\n\t"
      "v_pk_fma_f32 v[242:243], v[242:243], v[248:249], v[250:251]\n\t"
      "v_pk_fma_f32 v[244:245], v[244:245], v[248:249], v[250:251]\n\t"
      "v_pk_fma_f32 v[246:247], v[246:247], v[248:249], v[250:251]\n\t"
      "v_pk_mul_f32 v[240:241], v[240:241], v[128:129]\n\t"
      "v_pk_mul_f32 v[242:243], v[242:243], v[130:131]\n\t"
      "v_pk_mul_f32 v[244:245], v[244:245], v[132:133]\n\t"
      "v_pk_mul_f32 v[246:247], v[246:247], v[134:135]\n\t"
      "v_pk_mul_f32 v[136:137], v[128:129], v[136:137]\n\t"
      "v_pk_mul_f32 v[138:139], v[130:131], v[138:139]\n\t"
      "v_pk_mul_f32 v[140:141], v[132:133], v[140:141]\n\t"
      "v_pk_mul_f32 v[142:143], v[134:135], v[142:143]\n\t"
      "v_exp_f32 v240, v240\n\t"
      "v_exp_f32 v241, v241\n\t"
      "v_exp_f32 v242, v242\n\t"
      "v_exp_f32 v243, v243\n\t"
      "v_exp_f32 v244, v244\n\t"
      "v_exp_f32 v245, v245\n\t"
      "v_exp_f32 v246, v246\n\t"
      "v_exp_f32 v247, v247\n\t"
      "v_pk_add_f32 v[240:241], v[240:241], 1.0 op_sel_hi:[1,0]\n\t"
      "v_pk_add_f32 v[242:243], v[242:243], 1.0 op_sel_hi:[1,0]\n\t"
      "v_pk_add_f32 v[244:245], v[244:245], 1.0 op_sel_hi:[1,0]\n\t"
      "v_pk_add_f32 v[246:247], v[246:247], 1.0 op_sel_hi:[1,0]\n\t"
      "v_rcp_f32 v240, v240\n\t"
      "v_rcp_f32 v241, v241\n\t"
      "v_rcp_f32 v242, v242\n\t"
      "v_rcp_f32 v243, v243\n\t"
      "v_rcp_f32 v244, v244\n\t"
      "v_rcp_f32 v245, v245\n\t"
      "v_rcp_f32 v246, v246\n\t"
      "v_rcp_f32 v247, v247\n\t"
      "v_pk_mul_f32 v[136:137], v[136:137], v[240:241]\n\t"
      "v_pk_mul_f32 v[138:139], v[138:139], v[242:243]\n\t"
      "v_pk_mul_f32 v[140:141], v[140:141], v[244:245]\n\t"
      "v_pk_mul_f32 v[142:143], v[142:143], v[246:247]\n\t"
      "v_cvt_pk_bf16_f32 v128, v136, v137\n\t"
      "v_cvt_pk_bf16_f32 v130, v140, v141\n\t"
      "v_cvt_pk_bf16_f32 v129, v138, v139\n\t"
      "v_cvt_pk_bf16_f32 v131, v142, v143\n\t"
      "s_nop 0\n\t"
      "v_permlane16_swap_b32 v128, v130\n\t"
      "v_permlane16_swap_b32 v129, v131\n\t"
      "buffer_store_dwordx4 v[128:131], v49, s[4:7], s93 offen sc1\n\t"
      "v_pk_mul_f32 v[240:241], v[144:145], v[144:145]\n\t"
      "v_pk_mul_f32 v[242:243], v[146:147], v[146:147]\n\t"
      "v_pk_mul_f32 v[244:245], v[148:149], v[148:149]\n\t"
      "v_pk_mul_f32 v[246:247], v[150:151], v[150:151]\n\t"
      "v_pk_fma_f32 v[240:241], v[240:241], v[248:249], v[250:251]\n\t"
      "v_pk_fma_f32 v[242:243], v[242:243], v[248:249], v[250:251]\n\t"
      "v_pk_fma_f32 v[244:245], v[244:245], v[248:249], v[250:251]\n\t"
      "v_pk_fma_f32 v[246:247], v[246:247], v[248:249], v[250:251]\n\t"
      "v_pk_mul_f32 v[240:241], v[240:241], v[144:145]\n\t"
      "v_pk_mul_f32 v[242:243], v[242:243], v[146:147]\n\t"
      "v_pk_mul_f32 v[244:245], v[244:245], v[148:149]\n\t"
      "v_pk_mul_f32 v[246:247], v[246:247], v[150:151]\n\t"
      "v_pk_mul_f32 v[152:153], v[144:145], v[152:153]\n\t"
      "v_pk_mul_f32 v[154:155], v[146:147], v[154:155]\n\t"
      "v_pk_mul_f32 v[156:157], v[148:149], v[156:157]\n\t"
      "v_pk_mul_f32 v[158:159], v[150:151], v[158:159]\n\t"
      "v_exp_f32 v240, v240\n\t"
      "v_exp_f32 v241, v241\n\t"
      "v_exp_f32 v242, v242\n\t"
      "v_exp_f32 v243, v243\n\t"
      "v_exp_f32 v244, v244\n\t"
      "v_exp_f32 v245, v245\n\t"
      "v_exp_f32 v246, v246\n\t"
      "v_exp_f32 v247, v247\n\t"
      "v_pk_add_f32 v[240:241], v[240:241], 1.0 op_sel_hi:[1,0]\n\t"
      "v_pk_add_f32 v[242:243], v[242:243], 1.0 op_sel_hi:[1,0]\n\t"
      "v_pk_add_f32 v[244:245], v[244:245], 1.0 op_sel_hi:[1,0]\n\t"
      "v_pk_add_f32 v[246:247], v[246:247], 1.0 op_sel_hi:[1,0]\n\t"
      "v_rcp_f32 v240, v240\n\t"
      "v_rcp_f32 v241, v241\n\t"
      "v_rcp_f32 v242, v242\n\t"
      "v_rcp_f32 v243, v243\n\t"
      "v_rcp_f32 v244, v244\n\t"
      "v_rcp_f32 v245, v245\n\t"
      "v_rcp_f32 v246, v246\n\t"
      "v_rcp_f32 v247, v247\n\t"
      "v_pk_mul_f32 v[152:153], v[152:153], v[240:241]\n\t"
      "v_pk_mul_f32 v[154:155], v[154:155], v[242:243]\n\t"
      "v_pk_mul_f32 v[156:157], v[156:157], v[244:245]\n\t"
      "v_pk_mul_f32 v[158:159], v[158:159], v[246:247]\n\t"
      "v_cvt_pk_bf16_f32 v144, v152, v153\n\t"
      "v_cvt_pk_bf16_f32 v146, v156, v157\n\t"
      "v_cvt_pk_bf16_f32 v145, v154, v155\n\t"
      "v_cvt_pk_bf16_f32 v147, v158, v159\n\t"
      "s_nop 0\n\t"
      "v_permlane16_swap_b32 v144, v146\n\t"
      "v_permlane16_swap_b32 v145, v147\n\t"
      "buffer_store_dwordx4 v[144:147], v49, s[4:7], s94 offen sc1\n\t"
      "s_waitcnt vmcnt(3)\n\t"
      "s_call_b64 s[98:99], Lmval_%=\n\t"
      "s_mov_b32 exec_lo, s95\n\t"
      "s_mov_b32 exec_hi, 0\n\t"
      "global_atomic_add v252, v50, s[18:19] sc1\n\t"
      "s_mov_b64 exec, -1\n\t"
      "s_call_b64 s[98:99], Lsetupb_%=\n\t"
      "s_call_b64 s[98:99], Lsetupc_%=\n\t"
      "s_lshl_b32 exec_lo, s95, 4\n\t"
      "s_mov_b32 exec_hi, 0\n\t"
      "s_waitcnt vmcnt(1)\n\t"
      "global_atomic_add v252, v50, s[18:19] sc1\n\t"
      "s_mov_b64 exec, -1\n\t"
      "s_barrier\n\t"
      "Lbwd_%=:\n\t"
      "s_add_u32 s44, s42, 3\n\t"
      "s_and_b32 s44, s44, 3\n\t"
      "s_mul_i32 s44, s44, 81920\n\t"
      "s_add_u32 s44, s44, s62\n\t"
      "s_mul_i32 s45, s43, 10240\n\t"
      "s_add_u32 s45, s45, 40960\n\t"
      "s_add_u32 m0, s45, 0\n\t"
      "s_nop 0\n\t"
      "buffer_load_dwordx4 v2, s[28:31], s44 offen offset:0 lds\n\t"
      "buffer_load_dwordx4 v11, s[28:31], s44 offen offset:1024 lds\n\t"
      "buffer_load_dwordx4 v12, s[28:31], s44 offen offset:2048 lds\n\t"
      "buffer_load_dwordx4 v13, s[28:31], s44 offen offset:3072 lds\n\t"
      "s_add_u32 m0, s45, 4096\n\t"
      "s_add_u32 s44, s44, 32768\n\t"
      "buffer_load_dwordx4 v2, s[28:31], s44 offen offset:0 lds\n\t"
      "buffer_load_dwordx4 v11, s[28:31], s44 offen offset:1024 lds\n\t"
      "buffer_load_dwordx4 v12, s[28:31], s44 offen offset:2048 lds\n\t"
      "buffer_load_dwordx4 v13, s[28:31], s44 offen offset:3072 lds\n\t"
      "s_add_u32 m0, s45, 8192\n\t"
      "s_add_u32 s44, s44, 32768\n\t"
      "buffer_load_dwordx4 v2, s[28:31], s44 offen offset:0 lds\n\t"
      "buffer_load_dwordx4 v11, s[28:31], s44 offen offset:1024 lds\n\t"
      "s_add_u32 s46, s63, 1\n\t"
      "v_mov_b32 v190, s46\n\t"
      "s_lshl_b64 exec, 1, s34\n\t"
      "global_atomic_add v190, v252, v193, s[18:19] sc0 sc1\n\t"
      "s_mov_b64 exec, -1\n\t"
      "s_waitcnt vmcnt(0)\n\t"
      "s_branch Lp2_%=\n\t"
      "Lidle_%=:\n\t"
      "s_waitcnt lgkmcnt(0)\n\t"
      "s_barrier\n\t"
      "s_barrier\n\t"
      "s_barrier\n\t"
      "s_barrier\n\t"
      "s_barrier\n\t"
      "s_barrier\n\t"
      "ds_read_b64_tr_b16 v[32:33], v9 offset:0\n\t"
      "ds_read_b64_tr_b16 v[34:35], v9 offset:512\n\t"
      "ds_read_b64_tr_b16 v[36:37], v10 offset:0\n\t"
      "ds_read_b64_tr_b16 v[38:39], v10 offset:512\n\t"
      "ds_read_b64_tr_b16 v[40:41], v9 offset:2048\n\t"
      "ds_read_b64_tr_b16 v[42:43], v9 offset:2560\n\t"
      "ds_read_b64_tr_b16 v[44:45], v10 offset:2048\n\t"
      "ds_read_b64_tr_b16 v[46:47], v10 offset:2560\n\t"
      "s_waitcnt lgkmcnt(0)\n\t"
      "s_call_b64 s[98:99], Lsetupb_%=\n\t"
      "s_call_b64 s[98:99], Lsetupc_%=\n\t"
      "s_barrier\n\t"
      "s_barrier\n\t"
      "s_mov_b32 exec_lo, s35\n\t"
      "s_mov_b32 exec_hi, 0\n\t"
      "global_load_dwordx2 v[52:53], v253, s[18:19] sc1\n\t"
      "global_load_dword v254, v255, s[18:19] sc1\n\t"
      "s_mov_b64 exec, -1\n\t"
      "s_waitcnt vmcnt(0)\n\t"
      "s_call_b64 s[98:99], Lmval_%=\n\t"
      "s_barrier\n\t"
      "s_branch Lbwd_%=\n\t"
      "Lend_%=:\n\t"
      :
      : [ka] "s"(__builtin_amdgcn_kernarg_segment_ptr()), [bid] "s"(__builtin_amdgcn_workgroup_id_x()), [tid] "v"(__builtin_amdgcn_workitem_id_x())
      : "memory", "scc", "vcc", "v1", "v2", "v3", "v4", "v5", "v6", "v7", "v8", "v9", "v10", "v11", "v12", "v13", "v14", "v15", "v16", "v17", "v18", "v19", "v20", "v21", "v22", "v23", "v24", "v25", "v26", "v27", "v28", "v29", "v30", "v31", "v32", "v33", "v34", "v35", "v36", "v37", "v38", "v39", "v40", "v41", "v42", "v43", "v44", "v45", "v46", "v47", "v48", "v49", "v50", "v51", "v52", "v53", "v54", "v55", "v56", "v57", "v58", "v59", "v60", "v61", "v62", "v63", "v64", "v65", "v66", "v67", "v68", "v69", "v70", "v71", "v72", "v73", "v74", "v75", "v76", "v77", "v78", "v79", "v80", "v81", "v82", "v83", "v84", "v85", "v86", "v87", "v88", "v89", "v90", "v91", "v92", "v93", "v94", "v95", "v96", "v97", "v98", "v99", "v100", "v101", "v102", "v103", "v104", "v105", "v106", "v107", "v108", "v109", "v110", "v111", "v112", "v113", "v114", "v115", "v116", "v117", "v118", "v119", "v120", "v121", "v122", "v123", "v124", "v125", "v126", "v127", "v128", "v129", "v130", "v131", "v132", "v133", "v134", "v135", "v136", "v137", "v138", "v139", "v140", "v141", "v142", "v143", "v144", "v145", "v146", "v147", "v148", "v149", "v150", "v151", "v152", "v153", "v154", "v155", "v156", "v157", "v158", "v159", "v160", "v161", "v162", "v163", "v164", "v165", "v166", "v167", "v168", "v169", "v170", "v171", "v172", "v173", "v174", "v175", "v176", "v177", "v178", "v179", "v180", "v181", "v182", "v183", "v184", "v185", "v186", "v187", "v188", "v189", "v190", "v191", "v192", "v193", "v194", "v195", "v196", "v197", "v198", "v199", "v200", "v201", "v202", "v203", "v204", "v205", "v206", "v207", "v208", "v209", "v210", "v211", "v212", "v213", "v214", "v215", "v216", "v217", "v218", "v219", "v220", "v221", "v222", "v223", "v224", "v225", "v226", "v227", "v228", "v229", "v230", "v231", "v232", "v233", "v234", "v235", "v236", "v237", "v238", "v239", "v240", "v241", "v242", "v243", "v244", "v245", "v246", "v247", "v248", "v249", "v250", "v251", "v252", "v253", "v254", "v255", "s4", "s5", "s6", "s7", "s8", "s9", "s10", "s11", "s12", "s13", "s14", "s15", "s16", "s17", "s18", "s19", "s20", "s21", "s22", "s23", "s24", "s25", "s26", "s27", "s28", "s29", "s30", "s31", "s34", "s35", "s36", "s37", "s38", "s39", "s40", "s41", "s42", "s43", "s44", "s45", "s46", "s47", "s48", "s49", "s50", "s51", "s52", "s53", "s54", "s55", "s56", "s57", "s58", "s59", "s60", "s61", "s62", "s63", "s64", "s65", "s66", "s67", "s68", "s69", "s70", "s71", "s72", "s73", "s74", "s75", "s76", "s77", "s78", "s79", "s80", "s81", "s82", "s83", "s84", "s85", "s86", "s87", "s88", "s89", "s90", "s91", "s92", "s93", "s94", "s95", "s96", "s97", "s98", "s99");
}

extern "C" void kernel_launch(void* const* d_in, const int* in_sizes, int n_in, void* d_out, int out_size, void* d_ws,
                              size_t ws_size, hipStream_t stream) {
  (void)hipFuncSetAttribute((const void*)geglu_fused, hipFuncAttributeMaxDynamicSharedMemorySize, 163840);
  geglu_fused<<<256, 512, 163840, stream>>>(d_in[0], d_in[1], d_in[2], d_in[3], d_out, d_ws);
}
```
